```python
import torch

def _softmax(x, axis=-1):
    return torch.softmax(x, dim=axis)
def _gelu(x):
    return torch.nn.functional.gelu(x, approximate="tanh")
def _silu(x):
    return torch.nn.functional.silu(x)
def _sigmoid(x):
    return torch.sigmoid(x)
def _layernorm(x, axis=-1, eps=1e-5):
    m = x.mean(dim=axis, keepdim=True)
    v = x.var(dim=axis, keepdim=True, unbiased=False)
    return (x - m) / torch.sqrt(v + eps)
def _scatter_add(tgt, idx, src, axis=0):
    return tgt.clone().index_add_(axis, idx, src)
def _segment_sum(src, seg, n):
    out = torch.zeros((n,) + src.shape[1:], dtype=src.dtype)
    return out.index_add_(0, seg, src)
def _pad(x, pw):
    flat = tuple(v for pair in reversed(pw) for v in pair)
    return torch.nn.functional.pad(x, flat)

kernel_name = 'concat_heads__spectral_gfnet__ssm_conv_h3__prompt'

def setup_inputs():

    _w = (lambda a: (lambda t: t.to(torch.bfloat16) if t.is_floating_point() else t)(torch.as_tensor(a)))
    def _A_setup():
        B, S, D = 1, 2048, 640
        Df = D // 2 + 1
        return {
            "x": _w((lambda *s: torch.randn(*s, dtype=torch.bfloat16) * 0.05)(B, S, D)),
            "w_r": _w((lambda *s: torch.randn(*s, dtype=torch.bfloat16) * 0.05)(S, Df) * 0.02),
            "w_i": _w((lambda *s: torch.randn(*s, dtype=torch.bfloat16) * 0.05)(S, Df) * 0.02),
        }
    _A_in = _A_setup()

    def _B_setup():
        B, S, D = 1, 2048, 640
        ks = 4
        return {
            "q": _w((lambda *s: torch.randn(*s, dtype=torch.bfloat16) * 0.05)(B, S, D)),
            "k": _w((lambda *s: torch.randn(*s, dtype=torch.bfloat16) * 0.1)(B, S, D)),
            "v": _w((lambda *s: torch.randn(*s, dtype=torch.bfloat16) * 0.1)(B, S, D)),
            "shift_k": _w((lambda *s: torch.randn(*s, dtype=torch.bfloat16) * 0.1)(D, ks) * 0.5),
            "ssm_k": _w((lambda *s: torch.randn(*s, dtype=torch.bfloat16) * 0.1)(D, S) * (1.0 / S ** 0.5)),
            "ssm_D": _w((lambda *s: torch.randn(*s, dtype=torch.bfloat16) * 0.1)(D)),
        }
    _B_in = _B_setup()

    return {
        "A_x": _A_in["x"],
        "A_w_r": _A_in["w_r"],
        "A_w_i": _A_in["w_i"],
        "B_q": _B_in["q"],
        "B_k": _B_in["k"],
        "B_v": _B_in["v"],
        "B_shift_k": _B_in["shift_k"],
        "B_ssm_k": _B_in["ssm_k"],
        "B_ssm_D": _B_in["ssm_D"],
    }

def reference(A_x, A_w_r, A_w_i, B_q, B_k, B_v, B_shift_k, B_ssm_k, B_ssm_D):
    def _A_ref(x, w_r, w_i):
        B, S, D = x.shape
        X = (lambda x, axes=(-2,-1): torch.fft.rfft2(x.float(), dim=axes))(x, axes=(-2, -1))
        W = w_r + 1j * w_i
        X = X * W
        y = (lambda x, s, axes=(-2,-1): torch.fft.irfft2(x, s=s, dim=axes).to(torch.bfloat16))(X, (S, D), axes=(-2, -1))
        return y

    def _B_ref(q, k, v, shift_k, ssm_k, ssm_D):
        B, S, D = q.shape
        ks = shift_k.shape[-1]
        k_pad = _pad(k, ((0, 0), (ks - 1, 0), (0, 0)))
        k_shift = torch.zeros_like(k)
        for t in range(ks):
            k_shift = k_shift + k_pad[:, t:t + S, :] * shift_k[:, ks - 1 - t][None, None, :]
        kv = k_shift * v
        L = 2 * S
        kv_pad = _pad(kv, ((0, 0), (0, S), (0, 0)))
        kf_pad = _pad(ssm_k, ((0, 0), (0, S)))
        kv_f = (lambda x, axes=(-2,-1): torch.fft.rfft2(x.float(), dim=axes))(kv_pad[:, None, :, :], axes=(1, 2))
        kf_f = (lambda x, axes=(-2,-1): torch.fft.rfft2(x.float(), dim=axes))(kf_pad[:, None, :], axes=(1, 2))
        kf_f = (lambda x, *d: x.permute(*d))(kf_f, 1, 2, 0)
        y_f = kv_f * kf_f[None, :, :, :]
        y = (lambda x, s, axes=(-2,-1): torch.fft.irfft2(x, s=s, dim=axes).to(torch.bfloat16))(y_f, (1, L), axes=(1, 2))[:, 0, :S, :]
        y = y + kv * ssm_D[None, None, :]
        return q * y

    a = _A_ref(A_x, A_w_r, A_w_i)
    b = _B_ref(B_q, B_k, B_v, B_shift_k, B_ssm_k, B_ssm_D)
    return torch.concatenate([a, b], axis=-1)
```

```cpp
#include <hip/hip_runtime.h>
#include <hip/hip_bf16.h>
#include <stdint.h>

#define S_ 2048
#define D_ 640
#define DF_ 321

static __device__ __forceinline__ float bf2f(uint16_t v){ return __uint_as_float(((uint32_t)v)<<16); }
static __device__ __forceinline__ uint16_t f2bf(float f){ __hip_bfloat16 b = __float2bfloat16(f); return *(uint16_t*)&b; }
static __device__ __forceinline__ float rbf(float f){ return bf2f(f2bf(f)); }

__global__ void k1(const uint16_t* x, float2* X1){
  int kd = blockIdx.x*blockDim.x+threadIdx.x; int s = blockIdx.y;
  if(kd>=DF_) return;
  float re=0,im=0;
  for(int d=0;d<D_;d++){ int m=(d*kd)%D_; float sn,cs; sincospif(-2.0f*m/D_,&sn,&cs); float v=bf2f(x[s*D_+d]); re+=v*cs; im+=v*sn; }
  X1[s*DF_+kd]=make_float2(re,im);
}
__global__ void k2(const float2* in, float2* out, const uint16_t* wr, const uint16_t* wi, float sign, int usew){
  __shared__ float2 tw[S_];
  for(int i=threadIdx.x;i<S_;i+=blockDim.x){ float sn,cs; sincospif(sign*2.0f*i/S_,&sn,&cs); tw[i]=make_float2(cs,sn);}
  __syncthreads();
  int kd = blockIdx.x*blockDim.x+threadIdx.x; int ks = blockIdx.y;
  if(kd>=DF_) return;
  float re=0,im=0;
  for(int s=0;s<S_;s++){ float2 t=tw[(s*ks)&(S_-1)]; float2 v=in[s*DF_+kd]; re+=v.x*t.x-v.y*t.y; im+=v.x*t.y+v.y*t.x; }
  if(usew){ float a=bf2f(wr[ks*DF_+kd]), b=bf2f(wi[ks*DF_+kd]); float r2=re*a-im*b, i2=re*b+im*a; re=r2; im=i2; }
  out[ks*DF_+kd]=make_float2(re,im);
}
__global__ void k4(const float2* Z, uint16_t* out){
  int d = blockIdx.x*blockDim.x+threadIdx.x; int s = blockIdx.y;
  if(d>=D_) return;
  float acc = Z[s*DF_].x + ((d&1)?-1.f:1.f)*Z[s*DF_+320].x;
  for(int k=1;k<320;k++){ int m=(d*k)%D_; float sn,cs; sincospif(2.0f*m/D_,&sn,&cs); float2 v=Z[s*DF_+k]; acc+=2.f*(v.x*cs-v.y*sn); }
  out[s*1280+d]=f2bf(acc*(1.0f/(S_*(float)D_)));
}
__global__ void kb1(const uint16_t* k, const uint16_t* v, const uint16_t* sk, float* kv){
  int d = blockIdx.x*blockDim.x+threadIdx.x; int s = blockIdx.y;
  if(d>=D_) return;
  float acc=0;
  for(int t=0;t<4;t++){ int ss=s+t-3; float kk = ss>=0? bf2f(k[ss*D_+d]):0.f; acc = rbf(acc + rbf(kk*bf2f(sk[d*4+3-t]))); }
  kv[s*D_+d]=rbf(acc*bf2f(v[s*D_+d]));
}
__global__ void kb2(const float* kv, const uint16_t* q, const uint16_t* ssmk, const uint16_t* ssmD, uint16_t* out){
  int d = blockIdx.x*blockDim.x+threadIdx.x; int s = blockIdx.y;
  if(d>=D_) return;
  float acc=0;
  for(int t=0;t<=s;t++){ acc += kv[(s-t)*D_+d]*bf2f(ssmk[d*S_+t]); }
  float y=rbf(acc);
  y = rbf(y + rbf(kv[s*D_+d]*bf2f(ssmD[d])));
  out[s*1280+640+d]=f2bf(bf2f(q[s*D_+d])*y);
}

extern "C" void kernel_launch(void* const* d_in, const int* in_sizes, int n_in,
                              void* d_out, int out_size, void* d_ws, size_t ws_size,
                              hipStream_t stream) {
  const uint16_t* x=(const uint16_t*)d_in[0];
  const uint16_t* wr=(const uint16_t*)d_in[1];
  const uint16_t* wi=(const uint16_t*)d_in[2];
  const uint16_t* q=(const uint16_t*)d_in[3];
  const uint16_t* k=(const uint16_t*)d_in[4];
  const uint16_t* v=(const uint16_t*)d_in[5];
  const uint16_t* sk=(const uint16_t*)d_in[6];
  const uint16_t* ssmk=(const uint16_t*)d_in[7];
  const uint16_t* ssmD=(const uint16_t*)d_in[8];
  uint16_t* out=(uint16_t*)d_out;
  float2* A=(float2*)d_ws; float2* B=A+S_*DF_; float* kv=(float*)(B+S_*DF_);
  k1<<<dim3(6,S_),64,0,stream>>>(x,A);
  k2<<<dim3(6,S_),64,0,stream>>>(A,B,wr,wi,-1.f,1);
  k2<<<dim3(6,S_),64,0,stream>>>(B,A,wr,wi,1.f,0);
  k4<<<dim3(10,S_),64,0,stream>>>(A,out);
  kb1<<<dim3(10,S_),64,0,stream>>>(k,v,sk,kv);
  kb2<<<dim3(10,S_),64,0,stream>>>(kv,q,ssmk,ssmD,out);
}
```

```cpp
#include <hip/hip_runtime.h>
#include <stdint.h>

typedef __attribute__((ext_vector_type(2))) __bf16 bf2;
typedef __attribute__((ext_vector_type(8))) __bf16 bf8;
typedef __attribute__((ext_vector_type(2))) float f2;
typedef __attribute__((ext_vector_type(4))) float f4;
typedef __attribute__((ext_vector_type(16))) float f16v;
typedef __attribute__((ext_vector_type(2))) unsigned u2;
typedef __attribute__((ext_vector_type(4))) unsigned u4;
typedef __attribute__((ext_vector_type(4))) short s4;
#define DI __device__ __forceinline__
#define LDS __attribute__((address_space(3)))

constexpr int NCOL = 32 * 321;
constexpr unsigned SGN2 = 0x80008000u;

DI unsigned pk(float a, float b) { f2 v = {a, b}; return __builtin_bit_cast(unsigned, __builtin_convertvector(v, bf2)); }
DI float blo(unsigned v) { return __uint_as_float(v << 16); }
DI float bhi(unsigned v) { return __uint_as_float(v & 0xffff0000u); }
DI float fcos(float r) { return __builtin_amdgcn_cosf(r); }
DI float fsin(float r) { return __builtin_amdgcn_sinf(r); }
DI f4 mfma16(u4 a, u4 b, f4 c) { return __builtin_amdgcn_mfma_f32_16x16x32_bf16(__builtin_bit_cast(bf8, a), __builtin_bit_cast(bf8, b), c, 0, 0, 0); }
DI f16v mfma32(u4 a, u4 b, f16v c) { return __builtin_amdgcn_mfma_f32_32x32x16_bf16(__builtin_bit_cast(bf8, a), __builtin_bit_cast(bf8, b), c, 0, 0, 0); }
DI u2 trread(const char* p) { return __builtin_bit_cast(u2, __builtin_amdgcn_ds_read_tr16_b64_v4i16((LDS s4*)p)); }
DI u4 cat(u2 a, u2 b) { u4 r = {a[0], a[1], b[0], b[1]}; return r; }
DI u4 xr4(u4 a, unsigned m) { u4 r = {a[0] ^ m, a[1] ^ m, a[2] ^ m, a[3] ^ m}; return r; }

#define STAMP_ARG
#define STAMP_AT(k, i)

__global__ __launch_bounds__(640) void k1(const uint16_t* __restrict__ x, const uint16_t* __restrict__ kin, const uint16_t* __restrict__ vin,
                                          const uint16_t* __restrict__ shk, unsigned* __restrict__ T1, uint16_t* __restrict__ kvT STAMP_ARG) {
  __shared__ __attribute__((aligned(16))) char smem[12 * 1280 + 80 * 128];
  const int tid = threadIdx.x, l = tid & 63;
  const int w = __builtin_amdgcn_readfirstlane(tid >> 6);
  const int xcd = blockIdx.x & 7, idx = blockIdx.x >> 3;
  STAMP_AT(0, 0);
  if (w < 5) {
    const int q = idx & 3, s2 = xcd * 8 + (idx >> 2);
    const int i = l & 15, g = l >> 4;
    u4 xv[8];
    {
      const uint16_t* xp = x + (64 * 8 * g + s2) * 640 + 128 * w + 8 * i;
#pragma unroll
      for (int j = 0; j < 8; ++j) xv[j] = *(const u4*)(xp + j * 64 * 640);
    }
    u4 m1;
    {
      const int wv = i >> 1;
      const float kf = (wv < 4 ? (float)(4 * q + wv + 1) : 0.f) * (1.f / 2048.f);
      const float ph = 0.25f * (float)(i & 1);
      const float sb = (float)(512 * g + s2);
#pragma unroll
      for (int jj = 0; jj < 4; ++jj)
        m1[jj] = pk(fcos(fmaf(kf, sb + (float)(128 * jj), ph)), fcos(fmaf(kf, sb + (float)(128 * jj + 64), ph)));
    }
    const int k1i = l & 31, h = l >> 5;
    u4 C0, S0, PC, PS, MR, MI;
    float twc[12], tws[12];
    {
      const float kf = (float)k1i;
      const float a32 = kf * (1.f / 32.f);
#pragma unroll
      for (int jj = 0; jj < 4; ++jj) {
        const float a = a32 * (float)(8 * h + 2 * jj), b = a + a32;
        C0[jj] = pk(fcos(a), fcos(b)); S0[jj] = pk(fsin(a), fsin(b));
      }
      const float a640 = kf * (1.f / 640.f);
#pragma unroll
      for (int r = 0; r < 12; ++r) {
        const float a = a640 * (float)((r & 3) + 8 * (r >> 2) + 4 * h);
        twc[r] = fcos(a); tws[r] = fsin(a);
      }
      const int ro = (k1i & 3) + 4 * (k1i >> 3);
      const int k2 = (k1i & 4) ? 19 - ro : ro;
      const float a20 = (float)k2 * 0.05f;
#pragma unroll
      for (int jj = 0; jj < 4; ++jj) {
        const float a = a20 * (float)(8 * (jj >> 1) + 4 * h + 2 * (jj & 1)), b = a + a20;
        PC[jj] = pk(fcos(a), fcos(b)); PS[jj] = pk(fsin(a), fsin(b));
      }
      const unsigned msk = h ? 0u : 0xffffffffu;
#pragma unroll
      for (int jj = 0; jj < 2; ++jj) {
        const float a = a20 * (float)(16 + 2 * jj), b = a + a20;
        const unsigned c = pk(fcos(a), fcos(b)) & msk, s = pk(fsin(a), fsin(b)) & msk;
        MR[jj] = c; MR[2 + jj] = s; MI[jj] = s ^ SGN2; MI[2 + jj] = c;
      }
    }
    {
      f4 acc[8];
#pragma unroll
      for (int t = 0; t < 8; ++t) {
        u4 b;
#pragma unroll
        for (int jj = 0; jj < 4; ++jj)
          b[jj] = __builtin_amdgcn_perm(xv[2 * jj + 1][t >> 1], xv[2 * jj][t >> 1], (t & 1) ? 0x07060302u : 0x05040100u);
        const f4 z = {0.f, 0.f, 0.f, 0.f};
        acc[t] = mfma16(m1, b, z);
      }
      if (g < 3) {
#pragma unroll
        for (int r = 0; r < 4; ++r) {
          u4 o = {pk(acc[0][r], acc[1][r]), pk(acc[2][r], acc[3][r]), pk(acc[4][r], acc[5][r]), pk(acc[6][r], acc[7][r])};
          *(u4*)(smem + (4 * g + r) * 1280 + 256 * w + 16 * i) = o;
        }
      }
    }
    STAMP_AT(0, 1);
    __syncthreads();
    if (w < 4 || q == 0) {
      const int ks1 = w < 4 ? 4 * q + w + 1 : 0;
      const int grp = min(4 * ((l >> 4) & 1) + (l & 3), 4);
      const char* up = smem + 2 * w * 1280 + 40 * (8 * h + ((l >> 2) & 3)) + 8 * grp;
      u4 are[2], aim[2];
#pragma unroll
      for (int kk = 0; kk < 2; ++kk) {
        are[kk] = cat(trread(up + 640 * kk), trread(up + 640 * kk + 160));
        aim[kk] = cat(trread(up + 1280 + 640 * kk), trread(up + 1280 + 640 * kk + 160));
      }
      const unsigned sg = (k1i & 1) ? SGN2 : 0u;
      const u4 C1 = xr4(C0, sg), S1 = xr4(S0, sg);
      f16v vr = {}, vi = {};
      vr = mfma32(are[0], C0, vr); vi = mfma32(aim[0], C0, vi);
      vr = mfma32(aim[0], S0, vr); vi = mfma32(are[0], xr4(S0, SGN2), vi);
      vr = mfma32(are[1], C1, vr); vi = mfma32(aim[1], C1, vi);
      vr = mfma32(aim[1], S1, vr); vi = mfma32(are[1], xr4(S1, SGN2), vi);
      float pr[12], pi[12];
#pragma unroll
      for (int r = 0; r < 12; ++r) {
        pr[r] = vr[r] * twc[r] + vi[r] * tws[r];
        pi[r] = vi[r] * twc[r] - vr[r] * tws[r];
      }
      const u4 br = {pk(pr[0], pr[1]), pk(pr[2], pr[3]), pk(pr[4], pr[5]), pk(pr[6], pr[7])};
      const u4 bi = {pk(pi[0], pi[1]), pk(pi[2], pi[3]), pk(pi[4], pi[5]), pk(pi[6], pi[7])};
      const u4 bm = {pk(pr[8], pr[9]), pk(pr[10], pr[11]), pk(pi[8], pi[9]), pk(pi[10], pi[11])};
      f16v xr = {}, xi = {};
      xr = mfma32(PC, br, xr); xi = mfma32(PC, bi, xi);
      xr = mfma32(PS, bi, xr); xi = mfma32(xr4(PS, SGN2), br, xi);
      xr = mfma32(MR, bm, xr); xi = mfma32(MI, bm, xi);
      const float h0 = (l == 0) ? 0.5f : 1.f, h9 = (l == 32) ? 0.5f : 1.f;
      xr[0] *= h0; xi[0] *= h0; xr[9] *= h9; xi[9] *= h9;
      const unsigned cj = h ? 0x80000000u : 0u;
      const int cdir = (ks1 + 16) * 321, cmir = (16 - ks1) * 321;
      unsigned* tp = T1 + s2 * NCOL;
      unsigned o[10];
#pragma unroll
      for (int r = 0; r < 10; ++r) o[r] = pk(xr[r], xi[r]) ^ cj;
      if (h ? ks1 > 0 : ks1 < 16) {
        unsigned* p = tp + (h ? cmir + 32 - k1i : cdir + k1i);
#pragma unroll
        for (int r = 0; r < 10; ++r) p[32 * r] = o[r];
      }
      if (k1i == 0 && (h ? ks1 < 16 : ks1 > 0)) tp[h ? cdir + 320 : cmir] = (h ? o[9] : o[0]) ^ 0x80000000u;
    }
  } else {
    const int t = tid - 320, c8 = t % 10, rp = t / 10;
    const int r0 = 64 * idx + 2 * rp, ch0 = 80 * xcd + 8 * c8;
    u4 kr[5], vv[2], wk[4];
#pragma unroll
    for (int j = 0; j < 5; ++j) {
      const int r = r0 + j - 3;
      const u4 z = {0u, 0u, 0u, 0u};
      kr[j] = r >= 0 ? *(const u4*)(kin + r * 640 + ch0) : z;
    }
#pragma unroll
    for (int j = 0; j < 2; ++j) vv[j] = *(const u4*)(vin + (r0 + j) * 640 + ch0);
#pragma unroll
    for (int j = 0; j < 4; ++j) wk[j] = *(const u4*)(shk + ch0 * 4 + 8 * j);
    char* img = smem + 12 * 1280;
    const int pos = (31 - rp + 4 * c8) & 31;
#pragma unroll
    for (int e = 0; e < 8; ++e) {
      float kf[5], o[2];
#pragma unroll
      for (int j = 0; j < 5; ++j) kf[j] = (e & 1) ? bhi(kr[j][e >> 1]) : blo(kr[j][e >> 1]);
      const unsigned wa = wk[e >> 1][2 * (e & 1)], wb = wk[e >> 1][2 * (e & 1) + 1];
      const float w0 = blo(wa), w1 = bhi(wa), w2 = blo(wb), w3 = bhi(wb);
#pragma unroll
      for (int j = 0; j < 2; ++j) {
        const float ks = kf[j] * w3 + kf[j + 1] * w2 + kf[j + 2] * w1 + kf[j + 3] * w0;
        o[j] = ks * ((e & 1) ? bhi(vv[j][e >> 1]) : blo(vv[j][e >> 1]));
      }
      *(unsigned*)(img + (8 * c8 + e) * 128 + 4 * pos) = pk(o[1], o[0]);
    }
    __syncthreads();
#pragma unroll
    for (int j = 0; j < 2; ++j) {
      const int ci = t + 320 * j, ch = ci >> 3, k = ci & 7;
      const u4 v = *(const u4*)(img + ch * 128 + 16 * ((k + (ch >> 3)) & 7));
      *(u4*)(kvT + (80 * xcd + ch) * 2048 + 1984 - 64 * idx + 8 * k) = v;
    }
  }
  STAMP_AT(0, 2);
}

constexpr int NA2 = 168;
constexpr int KCS = 4160;

DI void dft64(const u4 (&ar)[4], const u4 (&ai)[4], const u4 (&b)[4], float (&xr)[16], float (&xi)[16]) {
  f4 er[4], ei[4];
  const f4 z = {0.f, 0.f, 0.f, 0.f};
#pragma unroll
  for (int c = 0; c < 4; ++c) { er[c] = mfma16(ar[c], b[c], z); ei[c] = mfma16(ai[c], b[c], z); }
#pragma unroll
  for (int r = 0; r < 4; ++r) {
    const float sr = er[0][r] + er[2][r], si = ei[0][r] + ei[2][r], dr = er[0][r] - er[2][r], di = ei[0][r] - ei[2][r];
    const float tr = er[1][r] + er[3][r], ti = ei[1][r] + ei[3][r], ur = er[1][r] - er[3][r], ui = ei[1][r] - ei[3][r];
    xr[r] = sr + tr;      xi[r] = si + ti;
    xr[4 + r] = dr + ui;  xi[4 + r] = di - ur;
    xr[8 + r] = sr - tr;  xi[8 + r] = si - ti;
    xr[12 + r] = dr - ui; xi[12 + r] = di + ur;
  }
}

__global__ __launch_bounds__(256) void k2(const unsigned* __restrict__ T1, const uint16_t* __restrict__ wr, const uint16_t* __restrict__ wi,
                                          unsigned* __restrict__ T2, const uint16_t* __restrict__ kvT, const uint16_t* __restrict__ ssk,
                                          const uint16_t* __restrict__ ssd, uint16_t* __restrict__ yT STAMP_ARG) {
  __shared__ __attribute__((aligned(16))) char smem[32768 + 4608];
  const int tid = threadIdx.x, l = tid & 63;
  const int w = __builtin_amdgcn_readfirstlane(tid >> 6);
  const int i = l & 15, g = l >> 4;
  STAMP_AT(1, 0);
  if (blockIdx.x < NA2) {
    const int tile = blockIdx.x * 4 + w;
    if (tile < NCOL / 16) {
      const int col = tile * 16 + i;
      u4 b[4];
      {
        const unsigned* p = T1 + 4 * g * NCOL + col;
#pragma unroll
        for (int c = 0; c < 4; ++c)
#pragma unroll
          for (int u = 0; u < 4; ++u) b[c][u] = p[(16 * u + c) * NCOL];
      }
      uint16_t wa[16], wb[16];
      {
        const int o = col - 16 * 321 + 4 * g * NCOL;
#pragma unroll
        for (int u = 0; u < 4; ++u)
#pragma unroll
          for (int r = 0; r < 4; ++r) {
            int oo = o + (16 * u + r) * NCOL;
            if (u == 0 && r == 0) oo += (oo < 0) ? 64 * NCOL : 0;
            wa[4 * u + r] = wr[oo]; wb[4 * u + r] = wi[oo];
          }
      }
      u4 ar[4], ai[4];
      {
        const float a64 = (float)i * (1.f / 64.f);
#pragma unroll
        for (int c = 0; c < 4; ++c)
#pragma unroll
          for (int u = 0; u < 4; ++u) {
            const float a = a64 * (float)(16 * u + c + 4 * g);
            const float cs = fcos(a), sn = fsin(a);
            ar[c][u] = pk(cs, sn); ai[c][u] = pk(-sn, cs);
          }
      }
      float xr[16], xi[16];
      dft64(ar, ai, b, xr, xi);
#pragma unroll
      for (int u = 0; u < 4; ++u)
#pragma unroll
        for (int r = 0; r < 4; ++r) {
          const int e = 4 * u + r;
          const float a = blo(wa[e]), bb = blo(wb[e]);
          b[r][u] = pk(xr[e] * a - xi[e] * bb, -(xr[e] * bb) - xi[e] * a);
        }
      dft64(ar, ai, b, xr, xi);
      unsigned* p = T2 + 4 * g * NCOL + col;
#pragma unroll
      for (int u = 0; u < 4; ++u)
#pragma unroll
        for (int r = 0; r < 4; ++r) p[(16 * u + r) * NCOL] = pk(xr[4 * u + r], xi[4 * u + r]);
    }
  } else {
    const int bb = blockIdx.x - NA2;
    const int ch = 80 * (bb & 7) + (bb >> 3);
    char* ul = smem + 32768;
    const u4 kc = *(const u4*)(ssk + ch * 2048 + 8 * tid);
    u2 kp = *(const u2*)(ssk + ch * 2048 + max(8 * tid - 4, 0));
    const u4 uv = *(const u4*)(kvT + ch * 2048 + 8 * tid);
    const float dv = blo(ssd[ch]);
    if (tid == 0) { kp[0] = 0u; kp[1] = 0u; }
    const u4 z4 = {0u, 0u, 0u, 0u};
    if (tid < 8) *(u4*)(smem + (tid >> 1) * KCS + 16 * (tid & 1)) = z4;
    if (tid < 32) *(u4*)(ul + 4096 + 16 * tid) = z4;
    {
      const unsigned o0 = __builtin_amdgcn_alignbit(kp[1], kp[0], 16), o1 = __builtin_amdgcn_alignbit(kc[0], kp[1], 16),
                     o2 = __builtin_amdgcn_alignbit(kc[1], kc[0], 16), o3 = __builtin_amdgcn_alignbit(kc[2], kc[1], 16),
                     o4 = __builtin_amdgcn_alignbit(kc[3], kc[2], 16);
      char* kp0 = smem + 32 + 16 * tid;
      const u4 c1 = {o1, o2, o3, o4}, c2 = {kp[1], kc[0], kc[1], kc[2]}, c3 = {o0, o1, o2, o3};
      *(u4*)(kp0) = kc; *(u4*)(kp0 + KCS) = c1; *(u4*)(kp0 + 2 * KCS) = c2; *(u4*)(kp0 + 3 * KCS) = c3;
      *(u4*)(ul + 16 * tid) = uv;
    }
    __syncthreads();
    STAMP_AT(1, 1);
    f4 acc[8];
#pragma unroll
    for (int j = 0; j < 8; ++j) acc[j] = (f4){0.f, 0.f, 0.f, 0.f};
    const char* ap = smem + ((3 - i) & 3) * KCS + 16 * g + 8 * (i >> 2) + 8 + 128 * w;
    const char* bp = ul + 4064 - 32 * i + 16 * g + 128 * w;
#pragma unroll 1
    for (int rr = 0; rr < 2; ++rr) {
      u4 a[8], b[8];
#pragma unroll
      for (int e = 0; e < 8; ++e) {
        a[e] = cat(*(const u2*)(ap + 512 * e), *(const u2*)(ap + 512 * e + 8));
        b[e] = *(const u4*)(bp - 512 * e);
      }
#pragma unroll
      for (int m = 0; m < 8; ++m)
#pragma unroll
        for (int e = 0; e + m < 8; ++e) acc[m + e] = mfma16(a[m], b[e], acc[m + e]);
      ap += 64; bp += 64;
    }
    __syncthreads();
#pragma unroll
    for (int j = 0; j < 8; ++j) *(f4*)(smem + (w * 8 + j) * 1024 + 16 * l) = acc[j];
    __syncthreads();
#pragma unroll
    for (int jj = 0; jj < 2; ++jj) {
      const int j = 2 * w + jj;
      f4 s = *(const f4*)(smem + j * 1024 + 16 * l);
#pragma unroll
      for (int ww = 1; ww < 4; ++ww) s += *(const f4*)(smem + (ww * 8 + j) * 1024 + 16 * l);
      const int n = 256 * j + 16 * i + 4 * g;
      const u2 kv = *(const u2*)(ul + 2 * (2044 - n));
      const u2 o = {pk(s[0] + dv * bhi(kv[1]), s[1] + dv * blo(kv[1])), pk(s[2] + dv * bhi(kv[0]), s[3] + dv * blo(kv[0]))};
      *(u2*)(yT + ch * 2048 + n) = o;
    }
  }
  STAMP_AT(1, 2);
}

constexpr int ZP = 768;
__global__ __launch_bounds__(832) void k3(const unsigned* __restrict__ T2, const uint16_t* __restrict__ yT, const uint16_t* __restrict__ qin,
                                          uint16_t* __restrict__ out STAMP_ARG) {
  __shared__ __attribute__((aligned(16))) char smem[16 * ZP + 80 * 144];
  const int tid = threadIdx.x, l = tid & 63;
  const int w = __builtin_amdgcn_readfirstlane(tid >> 6);
  const int xcd = blockIdx.x & 7, idx = blockIdx.x >> 3;
  const int i = l & 15, g = l >> 4;
  STAMP_AT(2, 0);
  if (w < 8) {
    const int q = idx & 3, s2 = xcd * 8 + (idx >> 2);
    u4 tv[8];
    if (w < 6) {
      const unsigned* p = T2 + s2 * NCOL + 4 * g * 321 + 64 * w + 4 * i;
#pragma unroll
      for (int j = 0; j < 8; ++j) tv[j] = *(const u4*)(p + (16 * (j >> 2) + (j & 3)) * 321);
    }
    const int da = l & 31, h = l >> 5;
    u4 C0, S0, QC, QS, QM;
    float twc[12], tws[12];
    const float scale = 2.f / (2048.f * 640.f);
    {
      const float kf = (float)da;
      const float a32 = kf * (1.f / 32.f);
#pragma unroll
      for (int jj = 0; jj < 4; ++jj) {
        const float a = a32 * (float)(8 * h + 2 * jj), b = a + a32;
        C0[jj] = pk(fcos(a), fcos(b)); S0[jj] = pk(fsin(a), fsin(b));
      }
      const float a640 = kf * (1.f / 640.f);
#pragma unroll
      for (int r = 0; r < 12; ++r) {
        const float a = a640 * (float)((r & 3) + 8 * (r >> 2) + 4 * h);
        twc[r] = fcos(a) * scale; tws[r] = fsin(a) * scale;
      }
      const float a20 = kf * 0.05f;
#pragma unroll
      for (int jj = 0; jj < 4; ++jj) {
        const float a = a20 * (float)(8 * (jj >> 1) + 4 * h + 2 * (jj & 1)), b = a + a20;
        QC[jj] = pk(fcos(a), fcos(b)); QS[jj] = pk(fsin(-a), fsin(-b));
      }
      const unsigned msk = h ? 0u : 0xffffffffu;
#pragma unroll
      for (int jj = 0; jj < 2; ++jj) {
        const float a = a20 * (float)(16 + 2 * jj), b = a + a20;
        QM[jj] = pk(fcos(a), fcos(b)) & msk; QM[2 + jj] = pk(fsin(-a), fsin(-b)) & msk;
      }
    }
    if (w < 6) {
      u4 m3[2];
      const float sf = (float)(64 * (8 * q + (i >> 1)) + s2) * (1.f / 2048.f);
      const float ph = -0.25f * (float)(i & 1);
#pragma unroll
      for (int kk = 0; kk < 2; ++kk)
#pragma unroll
        for (int jj = 0; jj < 4; ++jj) {
          const float a = fmaf(sf, (float)(16 * kk + jj - 16 + 4 * g), ph);
          m3[kk][jj] = pk(fcos(a), fsin(a));
        }
      f4 acc[4];
#pragma unroll
      for (int t = 0; t < 4; ++t) {
        const u4 b0 = {tv[0][t], tv[1][t], tv[2][t], tv[3][t]}, b1 = {tv[4][t], tv[5][t], tv[6][t], tv[7][t]};
        const f4 z = {0.f, 0.f, 0.f, 0.f};
        acc[t] = mfma16(m3[1], b1, mfma16(m3[0], b0, z));
      }
#pragma unroll
      for (int r = 0; r < 4; ++r) {
        const u2 o = {pk(acc[0][r], acc[1][r]), pk(acc[2][r], acc[3][r])};
        *(u2*)(smem + (4 * g + r) * ZP + 128 * w + 8 * i) = o;
      }
    }
    STAMP_AT(2, 1);
    __syncthreads();
    {
      const int grp = min(4 * ((l >> 4) & 1) + (l & 3), 4);
      const char* zp = smem + 2 * w * ZP + 40 * (8 * h + ((l >> 2) & 3)) + 8 * grp;
      const u4 are = cat(trread(zp), trread(zp + 160)), aim = cat(trread(zp + ZP), trread(zp + ZP + 160));
      const float nyq = blo(*(const uint16_t*)(smem + 2 * w * ZP + 640)) * scale;
      f16v vr = {}, vi = {};
      vr = mfma32(are, C0, vr); vi = mfma32(aim, C0, vi);
      vr = mfma32(aim, xr4(S0, SGN2), vr); vi = mfma32(are, S0, vi);
      float pr[12], pi[12];
#pragma unroll
      for (int r = 0; r < 12; ++r) {
        pr[r] = vr[r] * twc[r] - vi[r] * tws[r];
        pi[r] = vi[r] * twc[r] + vr[r] * tws[r];
      }
      const u4 br = {pk(pr[0], pr[1]), pk(pr[2], pr[3]), pk(pr[4], pr[5]), pk(pr[6], pr[7])};
      const u4 bi = {pk(pi[0], pi[1]), pk(pi[2], pi[3]), pk(pi[4], pi[5]), pk(pi[6], pi[7])};
      const u4 bm = {pk(pr[8], pr[9]), pk(pr[10], pr[11]), pk(pi[8], pi[9]), pk(pi[10], pi[11])};
      f16v y;
#pragma unroll
      for (int r = 0; r < 16; ++r) y[r] = (r & 1) ? -nyq : nyq;
      y = mfma32(br, QC, y); y = mfma32(bi, QS, y); y = mfma32(bm, QM, y);
      if (da < 20) {
        uint16_t* op = out + (64 * (8 * q + w) + s2) * 1280 + 32 * da + 4 * h;
#pragma unroll
        for (int t = 0; t < 4; ++t) {
          const u2 o = {pk(y[4 * t], y[4 * t + 1]), pk(y[4 * t + 2], y[4 * t + 3])};
          *(u2*)(op + 8 * t) = o;
        }
      }
    }
  } else {
    const int t = tid - 512;
    char* img = smem + 16 * ZP;
    u4 yv[2], qv[2];
#pragma unroll
    for (int j = 0; j < 2; ++j) {
      const int ci = t + 320 * j;
      yv[j] = *(const u4*)(yT + (80 * xcd + (ci >> 3)) * 2048 + 64 * idx + 8 * (ci & 7));
    }
    int blk[2], c8[2];
#pragma unroll
    for (int j = 0; j < 2; ++j) {
      const int gi = (t >> 4) + 20 * j;
      blk[j] = gi / 10; c8[j] = gi % 10;
      qv[j] = *(const u4*)(qin + (64 * idx + 16 * blk[j] + i) * 640 + 80 * xcd + 8 * c8[j]);
    }
#pragma unroll
    for (int j = 0; j < 2; ++j) {
      const int ci = t + 320 * j;
      *(u4*)(img + (ci >> 3) * 144 + 16 * (ci & 7)) = yv[j];
    }
    __syncthreads();
#pragma unroll
    for (int j = 0; j < 2; ++j) {
      const char* p = img + (8 * c8[j] + (i >> 2)) * 144 + 32 * blk[j] + 8 * (i & 3);
      const u4 y = cat(trread(p), trread(p + 4 * 144));
      u4 o;
#pragma unroll
      for (int e = 0; e < 4; ++e) o[e] = pk(blo(y[e]) * blo(qv[j][e]), bhi(y[e]) * bhi(qv[j][e]));
      *(u4*)(out + (64 * idx + 16 * blk[j] + i) * 1280 + 640 + 80 * xcd + 8 * c8[j]) = o;
    }
  }
  STAMP_AT(2, 2);
}

extern "C" void kernel_launch(void* const* d_in, const int* in_sizes, int n_in, void* d_out, int out_size, void* d_ws,
                              size_t ws_size, hipStream_t stream) {
  const uint16_t* x = (const uint16_t*)d_in[0];
  const uint16_t* wr = (const uint16_t*)d_in[1];
  const uint16_t* wi = (const uint16_t*)d_in[2];
  const uint16_t* q = (const uint16_t*)d_in[3];
  const uint16_t* k = (const uint16_t*)d_in[4];
  const uint16_t* v = (const uint16_t*)d_in[5];
  const uint16_t* shk = (const uint16_t*)d_in[6];
  const uint16_t* ssk = (const uint16_t*)d_in[7];
  const uint16_t* ssd = (const uint16_t*)d_in[8];
  uint16_t* out = (uint16_t*)d_out;
  unsigned* T2 = (unsigned*)d_ws;
  unsigned* T1 = T2 + 64 * NCOL;
  uint16_t* kvT = (uint16_t*)(T1 + 64 * NCOL);
  uint16_t* yT = kvT + 640 * 2048;
  k1<<<256, 640, 0, stream>>>(x, k, v, shk, T1, kvT);
  k2<<<NA2 + 640, 256, 0, stream>>>(T1, wr, wi, T2, kvT, ssk, ssd, yT);
  k3<<<256, 832, 0, stream>>>(T2, yT, q, out);
}
```

```cpp
#include <hip/hip_runtime.h>
#include <stdint.h>

typedef __attribute__((ext_vector_type(2))) __bf16 bf2;
typedef __attribute__((ext_vector_type(8))) __bf16 bf8;
typedef __attribute__((ext_vector_type(2))) float f2;
typedef __attribute__((ext_vector_type(4))) float f4;
typedef __attribute__((ext_vector_type(16))) float f16v;
typedef __attribute__((ext_vector_type(2))) unsigned u2;
typedef __attribute__((ext_vector_type(4))) unsigned u4;
typedef __attribute__((ext_vector_type(4))) short s4;
typedef __amdgpu_buffer_rsrc_t rsrc;
#define DI __device__ __forceinline__
#define LDS __attribute__((address_space(3)))
#define GLB __attribute__((address_space(1)))
#define PIN(x) asm volatile("" : "+v"(x))
#define SCHED() __builtin_amdgcn_sched_barrier(0)
#define CFENCE() asm volatile("" ::: "memory")
#define SPIN_LOOP _Pragma("unroll 1") for (unsigned spin = 0; spin < SPIN_MAX; ++spin)

#define WT_O 0
#define D_A2 20
#define D_A3 20
#define D_B2 20
#define D_B3 20
#define D_GO 24
#define D_TAB 10
#define D_W 60
constexpr int SC1 = 16;
constexpr int NCOL = 32 * 321;
constexpr int SLOT = 352;
constexpr int T1P = 32 * SLOT;
constexpr unsigned SGN2 = 0x80008000u;
constexpr int CODE_BYTES = 15360;
constexpr unsigned SPIN_MAX = 1u << 16;

constexpr int WS_T2 = 0, WS_T1 = 64 * NCOL * 4, WS_KV = WS_T1 + 64 * T1P * 4, WS_Y = WS_KV + 640 * 4096,
              WS_FLAG = WS_Y + 640 * 4096, FL_EPOCH = 16 * 1024;

struct Trig { double c[640]; };
constexpr Trig make_trig() {
  Trig t{};
  for (int m = 0; m <= 160; ++m) {
    const double x = 3.14159265358979323846 * m / 320.0, x2 = x * x;
    double term = 1.0, sum = 1.0;
    for (int k = 1; k <= 13; ++k) { term *= -x2 / ((2 * k - 1) * (2 * k)); sum += term; }
    if (m == 160) sum = 0.0;
    t.c[m] = sum; t.c[320 - m] = -sum; t.c[320 + m] = -sum; t.c[(640 - m) % 640] = sum;
  }
  return t;
}
constexpr Trig TRIG = make_trig();
constexpr double tcos(int m) { return TRIG.c[m % 640]; }
constexpr double tsin(int m) { return TRIG.c[(m + 480) % 640]; }
constexpr unsigned cbf(double v) { const unsigned u = __builtin_bit_cast(unsigned, (float)v); return (u + 0x7fffu + ((u >> 16) & 1u)) >> 16; }
constexpr unsigned cpk(double a, double b) { return cbf(a) | (cbf(b) << 16); }
enum { F_C0, F_S0, F_PC, F_PS, F_MR, F_TC, F_TS = F_TC + 3, F_QC = F_TS + 3, F_QS, F_QM, F_AR, F_ALL = F_AR + 4 };
struct Tab { unsigned v[F_ALL][64][4]; };
constexpr Tab make_tab() {
  Tab t{};
  for (int l = 0; l < 64; ++l) {
    const int n = l & 31, h = l >> 5;
    const int ro = (n & 3) + 4 * (n >> 3), k2 = (n & 4) ? 39 - ro : ro;
    for (int jj = 0; jj < 4; ++jj) {
      const int a = 20 * n * (8 * h + 2 * jj), b = a + 20 * n;
      t.v[F_C0][l][jj] = cpk(tcos(a), tcos(b));
      t.v[F_S0][l][jj] = cpk(tsin(a), tsin(b));
      const int d = 8 * (jj >> 1) + 4 * h + 2 * (jj & 1), dm = 16 + 2 * (jj & 1);
      t.v[F_PC][l][jj] = cpk(tcos(32 * k2 * d), tcos(32 * k2 * (d + 1)));
      t.v[F_PS][l][jj] = cpk(tsin(32 * k2 * d), tsin(32 * k2 * (d + 1)));
      t.v[F_MR][l][jj] = h ? 0u : (jj < 2 ? cpk(tcos(32 * k2 * dm), tcos(32 * k2 * (dm + 1))) : cpk(tsin(32 * k2 * dm), tsin(32 * k2 * (dm + 1))));
      t.v[F_QC][l][jj] = cpk(tcos(32 * n * d), tcos(32 * n * (d + 1)));
      t.v[F_QS][l][jj] = cpk(-tsin(32 * n * d), -tsin(32 * n * (d + 1)));
      t.v[F_QM][l][jj] = h ? 0u : (jj < 2 ? cpk(tcos(32 * n * dm), tcos(32 * n * (dm + 1))) : cpk(-tsin(32 * n * dm), -tsin(32 * n * (dm + 1))));
      for (int rg = 0; rg < 3; ++rg) {
        t.v[F_TC + rg][l][jj] = __builtin_bit_cast(unsigned, (float)tcos(n * (jj + 8 * rg + 4 * h)));
        t.v[F_TS + rg][l][jj] = __builtin_bit_cast(unsigned, (float)tsin(n * (jj + 8 * rg + 4 * h)));
      }
      for (int c = 0; c < 4; ++c) {
        const int e = 10 * (l & 15) * (16 * jj + c + 4 * (l >> 4));
        t.v[F_AR + c][l][jj] = cpk(tcos(e), tsin(e));
      }
    }
  }
  return t;
}
static __device__ const __attribute__((aligned(128))) Tab TAB = make_tab();

DI unsigned pk(float a, float b) { f2 v = {a, b}; return __builtin_bit_cast(unsigned, __builtin_convertvector(v, bf2)); }
DI unsigned pks(float a, float b) { return min(pk(a, b), 0xfffffffeu); }
DI float blo(unsigned v) { return __uint_as_float(v << 16); }
DI float bhi(unsigned v) { return __uint_as_float(v & 0xffff0000u); }
DI f2 unp(unsigned v) { f2 r = {blo(v), bhi(v)}; return r; }
DI float fcos(float r) { return __builtin_amdgcn_cosf(r); }
DI float fsin(float r) { return __builtin_amdgcn_sinf(r); }
DI f4 mfma16(u4 a, u4 b, f4 c) { return __builtin_amdgcn_mfma_f32_16x16x32_bf16(__builtin_bit_cast(bf8, a), __builtin_bit_cast(bf8, b), c, 0, 0, 0); }
DI f16v mfma32(u4 a, u4 b, f16v c) { return __builtin_amdgcn_mfma_f32_32x32x16_bf16(__builtin_bit_cast(bf8, a), __builtin_bit_cast(bf8, b), c, 0, 0, 0); }
DI u2 trread(const char* p) { return __builtin_bit_cast(u2, __builtin_amdgcn_ds_read_tr16_b64_v4i16((LDS s4*)p)); }
DI u4 cat(u2 a, u2 b) { u4 r = {a[0], a[1], b[0], b[1]}; return r; }
DI rsrc mkrs(const void* p, unsigned bytes) { return __builtin_amdgcn_make_buffer_rsrc((void*)p, 0, bytes, 0x00020000); }
template <int AUX = 0> DI u4 ld128(rsrc r, int v, int s) { return __builtin_bit_cast(u4, __builtin_amdgcn_raw_buffer_load_b128(r, v, s, AUX)); }
template <int AUX = 0> DI u2 ld64(rsrc r, int v, int s) { return __builtin_bit_cast(u2, __builtin_amdgcn_raw_buffer_load_b64(r, v, s, AUX)); }
template <int AUX = 0> DI unsigned ld32(rsrc r, int v, int s) { return __builtin_amdgcn_raw_buffer_load_b32(r, v, s, AUX); }
template <int AUX> DI void st128(u4 x, rsrc r, int v, int s) { __builtin_amdgcn_raw_buffer_store_b128(x, r, v, s, AUX); }
template <int AUX> DI void st32(unsigned x, rsrc r, int v, int s) { __builtin_amdgcn_raw_buffer_store_b32(x, r, v, s, AUX); }
DI unsigned umax4(unsigned m, u4 a) { return max(max(max(m, a[0]), a[1]), max(a[2], a[3])); }
DI void drain() { asm volatile("s_waitcnt vmcnt(0)" ::: "memory"); }
template <int H> DI f2 half(const f4& a) { return __builtin_shufflevector(a, a, 2 * H, 2 * H + 1); }
template <int H> DI f2 half(const f16v& a) { return __builtin_shufflevector(a, a, 2 * H, 2 * H + 1); }
template <int SA, int SB> DI f2 mad(f2 a, f2 b, f2 c, f2 d) {
  const f2 t = c * d;
  return __builtin_elementwise_fma(SA > 0 ? a : -a, b, SB > 0 ? t : -t);
}

DI void arrive(int off) {
  unsigned one;
  asm volatile("s_mov_b64 exec, 1\n\tv_mov_b32 %0, 1\n\tds_add_u32 %1, %0\n\ts_mov_b64 exec, -1" : "=&v"(one) : "v"(off) : "memory");
}
template <int SLEEP> DI void await(int off, int n) {
  unsigned v, s;
  asm volatile("1:\n\tds_read_b32 %0, %2\n\ts_waitcnt lgkmcnt(0)\n\tv_readfirstlane_b32 %1, %0\n\ts_cmp_ge_u32 %1, %3\n\t"
               "s_cbranch_scc1 2f\n\ts_sleep %4\n\ts_branch 1b\n2:" : "=&v"(v), "=&s"(s) : "v"(off), "i"(n), "i"(SLEEP) : "memory", "scc");
}

#define STAMP_ARG
#define STAMP_AT(i)

constexpr int KCS = 4160;
constexpr int G_P = 4 * KCS, G_U = G_P + 16384;
constexpr int GRP = G_U + 4608;
constexpr int ZP = 768;
constexpr int L_SYNC = 0, L_TAB = 128, L_U = L_TAB + F_ALL * 1024, L_IMG = L_U + 12 * 1280, L_GRP = L_IMG + 80 * 144, L_END = L_GRP + 3 * GRP;
enum { C_TAB, C_PRE, C_GO, C_PRE2, C_GO2, C_TAG, C_A1, C_A1D, C_B1, C_B1D, C_K, C_G = C_K + 3, C_A2D = C_G + 3, C_A3, C_B2D, C_W };
#define SY(c) (L_SYNC + 4 * (c))

template <int H> DI void bfly4(const f4 (&er)[4], const f4 (&ei)[4], f2 (&xr)[4][2], f2 (&xi)[4][2]) {
  const f2 sr = half<H>(er[0]) + half<H>(er[2]), si = half<H>(ei[0]) + half<H>(ei[2]);
  const f2 dr = half<H>(er[0]) - half<H>(er[2]), di = half<H>(ei[0]) - half<H>(ei[2]);
  const f2 tr = half<H>(er[1]) + half<H>(er[3]), ti = half<H>(ei[1]) + half<H>(ei[3]);
  const f2 ur = half<H>(er[1]) - half<H>(er[3]), ui = half<H>(ei[1]) - half<H>(ei[3]);
  xr[0][H] = sr + tr; xi[0][H] = si + ti;
  xr[1][H] = dr + ui; xi[1][H] = di - ur;
  xr[2][H] = sr - tr; xi[2][H] = si - ti;
  xr[3][H] = dr - ui; xi[3][H] = di + ur;
}
DI void dft64(const u4 (&ar)[4], const u4 (&ai)[4], const u4 (&b)[4], f2 (&xr)[4][2], f2 (&xi)[4][2]) {
  f4 er[4], ei[4];
  const f4 z = {0.f, 0.f, 0.f, 0.f};
#pragma unroll
  for (int c = 0; c < 4; ++c) { const int cc = (c >> 1) | ((c & 1) << 1); er[cc] = mfma16(ar[cc], b[cc], z); ei[cc] = mfma16(ai[cc], b[cc], z); }
  bfly4<0>(er, ei, xr, xi); bfly4<1>(er, ei, xr, xi);
}

template <bool INV, int J> DI void tw1(const f16v& vr, const f16v& vi, const f4 (&tc)[3], const f4 (&tn)[3], unsigned& pr, unsigned& pi) {
  const f2 a = half<J>(vr), b = half<J>(vi), c = half<J & 1>(tc[J >> 1]), s = half<J & 1>(tn[J >> 1]);
  const f2 re = mad<1, INV ? -1 : 1>(a, c, b, s), im = mad<1, INV ? 1 : -1>(b, c, a, s);
  pr = pk(re[0], re[1]); pi = pk(im[0], im[1]);
}
template <bool INV> DI void twiddle(const f16v& vr, const f16v& vi, const f4 (&tc)[3], const f4 (&tn)[3], u4& br, u4& bi, u4& bm) {
  unsigned pr[6], pi[6];
  tw1<INV, 0>(vr, vi, tc, tn, pr[0], pi[0]); tw1<INV, 1>(vr, vi, tc, tn, pr[1], pi[1]); tw1<INV, 2>(vr, vi, tc, tn, pr[2], pi[2]);
  tw1<INV, 3>(vr, vi, tc, tn, pr[3], pi[3]); tw1<INV, 4>(vr, vi, tc, tn, pr[4], pi[4]); tw1<INV, 5>(vr, vi, tc, tn, pr[5], pi[5]);
  br = (u4){pr[0], pr[1], pr[2], pr[3]}; bi = (u4){pi[0], pi[1], pi[2], pi[3]}; bm = (u4){pr[4], pr[5], pi[4], pi[5]};
}

template <int MASK> DI void conv_part(char* base, int ao, int bo, int wo, int cg) {
  constexpr int NB = (MASK & 0x80) ? 8 : 6;
  f4 acc[8];
#pragma unroll
  for (int j = 0; j < 8; ++j) acc[j] = (f4){0.f, 0.f, 0.f, 0.f};
  u4 a[2][NB], b[2][NB];
  auto rd = [&](int rr, int e) {
    b[rr][e] = *(const u4*)(base + bo + 512 * (7 - e) + 64 * rr);
    a[rr][e] = cat(trread(base + ao + 512 * e + 64 * rr), trread(base + ao + 512 * e + 64 * rr + 8));
    SCHED();
  };
  auto wr = [&](int j) { if ((MASK >> j) & 1) *(f4*)(base + wo + (j < 4 ? G_P + 4096 * j : 4096 * (j - 4))) = acc[j]; };
#pragma unroll
  for (int e = 0; e < NB; ++e) rd(0, e);
#pragma unroll
  for (int rr = 0; rr < 2; ++rr) {
#pragma unroll
    for (int s = 0; s < NB; ++s) {
#pragma unroll
      for (int e = 0; e <= s; ++e)
        if (s + e < 8 && ((MASK >> (s + e)) & 1)) acc[s + e] = mfma16(a[rr][s], b[rr][e], acc[s + e]);
#pragma unroll
      for (int e = 0; e < s; ++e)
        if (s + e < 8 && ((MASK >> (s + e)) & 1)) acc[s + e] = mfma16(a[rr][e], b[rr][s], acc[s + e]);
      SCHED();
      if (rr == 0) {
        if (2 * s >= NB) { rd(1, 2 * s - NB); rd(1, 2 * s - NB + 1); }
        if (s == NB - 1) arrive(cg);
      } else {
        if (s == 5) await<0>(cg, 8);
        if (s >= 1) wr(s - 1);
        SCHED();
      }
    }
  }
  wr(NB - 1);
}

template <int N> DI void fill(char* p, int bytes, int l) {
  const rsrc rp = mkrs(p, bytes);
  const u4 ff = {~0u, ~0u, ~0u, ~0u};
#pragma unroll
  for (int j = 0; j < N; ++j) st128<SC1>(ff, rp, 16 * l + 1024 * j, 0);
}
DI void flag_round(rsrc rf, int base, unsigned tag, int l, int xcd) {
  if (l < 8) st32<SC1>(tag, rf, base + 1024 * l + 4 * blockIdx.x, 0);
  SPIN_LOOP {
    __builtin_amdgcn_s_sleep(D_GO);
    CFENCE();
    const u4 f = ld128<SC1>(rf, base + 1024 * xcd + 16 * l, 0);
    if (__all(f[0] == tag && f[1] == tag && f[2] == tag && f[3] == tag)) break;
  }
}

extern "C" __global__ __launch_bounds__(1024) void gfh3(const uint16_t* __restrict__ x, const uint16_t* __restrict__ wr, const uint16_t* __restrict__ wi,
                                                        const uint16_t* __restrict__ qin, const uint16_t* __restrict__ kin, const uint16_t* __restrict__ vin,
                                                        const uint16_t* __restrict__ shk, const uint16_t* __restrict__ ssk, const uint16_t* __restrict__ ssd,
                                                        uint16_t* __restrict__ out, char* ws STAMP_ARG) {
  __shared__ __attribute__((aligned(16))) char smem[L_END];
  const int l = threadIdx.x & 63;
  const int w = (__builtin_amdgcn_readfirstlane(threadIdx.x >> 6) + 13) & 15;
  const int tid = 64 * w + l;
  const int xcd = blockIdx.x & 7, idx = blockIdx.x >> 3;
  const int q = idx & 3, s2 = xcd * 8 + (idx >> 2);
  const int i = l & 15, g = l >> 4, n32 = l & 31, h = l >> 5;
  const char* tab = smem + L_TAB + 16 * l;
#define FRAG(f) (*(const u4*)(tab + 1024 * (f)))
#define FRAGF(f) (*(const f4*)(tab + 1024 * (f)))
  STAMP_AT(0);
  unsigned sink = 0;
  if (w == 13) {
    unsigned long long pc; int rel;
    asm volatile("s_getpc_b64 %0\n\ts_mov_b32 %1, gfh3@rel32@lo+4" : "=s"(pc), "=s"(rel));
    const char* p = (const char*)(pc + (long long)rel) + 128 * l;
    asm volatile("global_load_dword %0, %1, off" : "+v"(sink) : "v"(p) : "memory");
    asm volatile("global_load_dword %0, %1, off" : "+v"(sink) : "v"(p + (CODE_BYTES - 8192)) : "memory");
    if (l < 32) *(unsigned*)(smem + L_SYNC + 4 * l) = 0u;
  }
  asm volatile("s_waitcnt lgkmcnt(0)\n\ts_barrier" :: "s"(x), "s"(wr), "s"(wi), "s"(qin), "s"(kin), "s"(vin), "s"(shk), "s"(ssk), "s"(ssd), "s"(out), "s"(ws) : "memory");
  bool conv = false;
  const rsrc rf = mkrs(ws + WS_FLAG, FL_EPOCH + 4);

  if (w < 5) {
    u4 xv[8];
    {
      const rsrc rx = mkrs(x + s2 * 640 + 128 * w, 0x7fffffff);
      const int vo = g * (512 * 1280) + 16 * i;
#pragma unroll
      for (int j = 0; j < 8; ++j) xv[j] = ld128(rx, vo, j * 64 * 1280);
    }
    SCHED();
    u4 m1;
    {
      const int wv = i >> 1;
      const float kf = (wv < 4 ? (float)(4 * q + wv + 1) : 0.f) * (1.f / 2048.f);
      const float ph = 0.25f * (float)(i & 1);
      const float sb = (float)(512 * g + s2);
#pragma unroll
      for (int jj = 0; jj < 4; ++jj)
        m1[jj] = pk(fcos(fmaf(kf, sb + (float)(128 * jj), ph)), fcos(fmaf(kf, sb + (float)(128 * jj + 64), ph)));
    }
    const int ks1 = w < 4 ? 4 * q + w + 1 : 0;
    const rsrc rt = mkrs(ws + WS_T1 + s2 * T1P * 4, T1P * 4);
    const int cm = (16 - ks1) * SLOT + (ks1 ? 0 : T1P), cd = (ks1 + 16) * SLOT;
    int so = 4 * (h ? cm + 63 - n32 : cd + n32);
    int sx = 4 * ((h ? cd + 320 : cm + 31) + (n32 ? T1P : 0));
    int upo = L_U + 2 * w * 1280 + 40 * (8 * h + ((l >> 2) & 3)) + 8 * min(4 * ((l >> 4) & 1) + (l & 3), 4);
    int wro = L_U + 4 * g * 1280 + 256 * w + 16 * i;
    PIN(m1); PIN(so); PIN(sx); PIN(upo); PIN(wro);
    STAMP_AT(1);
    {
      f4 acc[8];
#pragma unroll
      for (int t = 0; t < 8; ++t) {
        u4 b;
#pragma unroll
        for (int jj = 0; jj < 4; ++jj)
          b[jj] = __builtin_amdgcn_perm(xv[2 * jj + 1][t >> 1], xv[2 * jj][t >> 1], (t & 1) ? 0x07060302u : 0x05040100u);
        const f4 z = {0.f, 0.f, 0.f, 0.f};
        acc[t] = mfma16(m1, b, z);
      }
      if (g < 3) {
#pragma unroll
        for (int r = 0; r < 4; ++r) {
          u4 o = {pk(acc[0][r], acc[1][r]), pk(acc[2][r], acc[3][r]), pk(acc[4][r], acc[5][r]), pk(acc[6][r], acc[7][r])};
          *(u4*)(smem + wro + r * 1280) = o;
        }
      }
    }
    arrive(SY(C_A1));
    STAMP_AT(2);
    await<1>(SY(C_TAB), 1);
    if (w < 4 || q == 0) {
      const u4 C0 = FRAG(F_C0), S0 = FRAG(F_S0), PC = FRAG(F_PC), PS = FRAG(F_PS), MR = FRAG(F_MR);
      f4 tc[3], tn[3];
#pragma unroll
      for (int j = 0; j < 3; ++j) { tc[j] = FRAGF(F_TC + j); tn[j] = FRAGF(F_TS + j); }
      const unsigned sg = (n32 & 1) ? SGN2 : 0u;
      u4 N0 = S0 ^ SGN2, C1 = C0 ^ sg, S1 = S0 ^ sg, N1 = N0 ^ sg;
      PIN(N0); PIN(C1); PIN(S1); PIN(N1);
      await<0>(SY(C_A1), 5);
      const char* up = smem + upo;
      u4 are[2], aim[2];
#pragma unroll
      for (int kk = 0; kk < 2; ++kk) {
        are[kk] = cat(trread(up + 640 * kk), trread(up + 640 * kk + 160));
        aim[kk] = cat(trread(up + 1280 + 640 * kk), trread(up + 1280 + 640 * kk + 160));
      }
      f16v vr = {}, vi = {};
      vr = mfma32(are[0], C0, vr); vi = mfma32(are[0], N0, vi);
      vr = mfma32(aim[0], S0, vr); vi = mfma32(aim[0], C0, vi);
      vr = mfma32(are[1], C1, vr); vi = mfma32(are[1], N1, vi);
      vr = mfma32(aim[1], S1, vr); vi = mfma32(aim[1], C1, vi);
      const u4 PN = PS ^ SGN2, MI = {MR[2] ^ SGN2, MR[3] ^ SGN2, MR[0], MR[1]};
      u4 br, bi, bm;
      twiddle<false>(vr, vi, tc, tn, br, bi, bm);
      f16v xr = {}, xi = {};
      xr = mfma32(PC, br, xr); xi = mfma32(PN, br, xi);
      xr = mfma32(PS, bi, xr); xi = mfma32(PC, bi, xi);
      xr = mfma32(MR, bm, xr); xi = mfma32(MI, bm, xi);
      SCHED();
      await<1>(SY(C_GO), 1);
      const float h0 = (l == 0) ? 0.5f : 1.f, h9 = (l == 32) ? 0.5f : 1.f;
      xr[0] *= h0; xi[0] *= h0; xr[9] *= h9; xi[9] *= h9;
      const unsigned cj = h ? 0x80000000u : 0u;
      unsigned o[10];
#pragma unroll
      for (int r = 0; r < 10; ++r) { o[r] = pk(xr[r], xi[r]) ^ cj; st32<SC1>(min(o[r], 0xfffffffeu), rt, so + 128 * r, 0); }
      st32<SC1>(min((h ? o[9] : o[0]) ^ 0x80000000u, 0xfffffffeu), rt, sx, 0);
    }
    arrive(SY(C_A1D));
    STAMP_AT(3);
    if (w == 4) {
      const unsigned tag = *(const unsigned*)(smem + SY(C_TAG));
      await<2>(SY(C_PRE2), 2);
      flag_round(rf, 8192, tag, l, xcd);
      arrive(SY(C_GO2));
      STAMP_AT(4);
    }
    conv = w < 4;
  } else if (w < 8) {
    const int tile = ((321 * xcd) >> 2) + 3 * idx + (w - 5);
    const bool act = tile < ((321 * (xcd + 1)) >> 2);
    __builtin_amdgcn_s_sleep(D_W);
    u4 wv[4];
    {
      const rsrc ra = mkrs(wr, act ? 128 * NCOL : 0), rb = mkrs(wi, act ? 128 * NCOL : 0);
      int o = 2 * (16 * tile - 16 * 321 + (l >> 1) * NCOL) + 16 * (l & 1);
      const int o2 = o + 64 * NCOL;
      o += (o < 0) ? 128 * NCOL : 0;
      wv[0] = ld128(ra, o, 0); wv[1] = ld128(ra, o2, 0); wv[2] = ld128(rb, o, 0); wv[3] = ld128(rb, o2, 0);
    }
    if (w == 5) fill<11>(ws + WS_T2 + (4 * s2 + q) * NCOL, NCOL, l);
    if (w == 6) fill<10>(ws + WS_Y + (32 * xcd + idx) * 10240, 10240, l);
    char* wsc = smem + L_GRP + (w - 5) * GRP + G_P;
#pragma unroll
    for (int j = 0; j < 4; ++j) *(u4*)(wsc + 1024 * j + 16 * l) = wv[j];
    f2 wa[4][2], wb[4][2];
#pragma unroll
    for (int u = 0; u < 4; ++u) {
#pragma unroll
      for (int r = 0; r < 4; ++r) {
        const char* p = wsc + 128 * g + 2 * i + 512 * u + 32 * r;
        wa[u][r >> 1][r & 1] = blo(*(const uint16_t*)p); wb[u][r >> 1][r & 1] = blo(*(const uint16_t*)(p + 2048));
      }
      PIN(wa[u][0]); PIN(wb[u][0]); PIN(wa[u][1]); PIN(wb[u][1]);
    }
    arrive(SY(C_W));
    STAMP_AT(1);
    if (w < 7) { drain(); arrive(SY(C_PRE2)); }
    await<2>(SY(C_TAB), 1);
    u4 ar[4], ai[4];
#pragma unroll
    for (int c = 0; c < 4; ++c) {
      ar[c] = FRAG(F_AR + c);
#pragma unroll
      for (int u = 0; u < 4; ++u) ai[c][u] = __builtin_amdgcn_alignbit(ar[c][u], ar[c][u], 16) ^ 0x8000u;
      PIN(ar[c]); PIN(ai[c]);
    }
    const rsrc rt = mkrs(ws + WS_T1, 64 * T1P * 4), ro = mkrs(ws + WS_T2, 64 * NCOL * 4);
    const int col = tile * 16 + i, slot = col / 321;
    int vi = 4 * (4 * g * T1P + col + slot * (SLOT - 321) + (slot < 16 ? 31 : 0));
    int vo = 4 * (4 * g * NCOL + col);
    PIN(vi); PIN(vo);
    STAMP_AT(2);
    await<2>(SY(C_A1D), 5);
    __builtin_amdgcn_s_sleep(D_A2);
    if (act) {
      u4 b[4];
      SPIN_LOOP {
        CFENCE();
#pragma unroll
        for (int c = 0; c < 4; ++c)
#pragma unroll
          for (int u = 0; u < 4; ++u) b[c][u] = ld32<SC1>(rt, vi, (16 * u + c) * T1P * 4);
        const unsigned m = umax4(umax4(umax4(umax4(0u, b[0]), b[1]), b[2]), b[3]);
        if (__all(m != ~0u)) break;
      }
      STAMP_AT(3);
      __builtin_amdgcn_s_setprio(3);
      f2 xr[4][2], xi[4][2];
      dft64(ar, ai, b, xr, xi);
#pragma unroll
      for (int hh = 0; hh < 2; ++hh)
#pragma unroll
        for (int u = 0; u < 4; ++u) {
          const f2 re = mad<1, -1>(xr[u][hh], wa[u][hh], xi[u][hh], wb[u][hh]), im = mad<-1, -1>(xr[u][hh], wb[u][hh], xi[u][hh], wa[u][hh]);
          b[2 * hh][u] = pk(re[0], im[0]); b[2 * hh + 1][u] = pk(re[1], im[1]);
        }
      STAMP_AT(5);
      dft64(ar, ai, b, xr, xi);
      await<1>(SY(C_GO2), 1);
#pragma unroll
      for (int u = 0; u < 4; ++u)
#pragma unroll
        for (int r = 0; r < 4; ++r) st32<SC1>(pks(xr[u][r >> 1][r & 1], xi[u][r >> 1][r & 1]), ro, vo, (16 * u + r) * NCOL * 4);
      __builtin_amdgcn_s_setprio(0);
    } else {
      __builtin_amdgcn_s_sleep(40);
      await<1>(SY(C_GO2), 1);
    }
    arrive(SY(C_A2D));
    STAMP_AT(4);
  } else if (w < 13) {
    const int t = tid - 512, c2 = t % 40, tc = t / 40;
    unsigned kr[11], vv[8];
    u4 wk;
    __builtin_amdgcn_s_setprio(1);
    {
      const rsrc rk = mkrs(kin, 2048 * 1280), rv = mkrs(vin, 2048 * 1280), rs = mkrs(shk, 640 * 8);
      const int vo = (64 * idx + 8 * tc) * 1280 + 160 * xcd + 4 * c2;
      const int vh = (idx | tc) ? vo - 3 * 1280 : 2048 * 1280;
      wk = ld128(rs, 640 * xcd + 16 * c2, 0);
#pragma unroll
      for (int j = 0; j < 8; ++j) { kr[3 + j] = ld32(rk, vo, j * 1280); vv[j] = ld32(rv, vo, j * 1280); }
#pragma unroll
      for (int j = 0; j < 3; ++j) kr[j] = ld32(rk, vh + j * 1280, 0);
    }
    SCHED();
    char* img = smem + L_IMG;
    STAMP_AT(1);
    {
      const f2 w0 = {blo(wk[0]), blo(wk[2])}, w1 = {bhi(wk[0]), bhi(wk[2])}, w2 = {blo(wk[1]), blo(wk[3])}, w3 = {bhi(wk[1]), bhi(wk[3])};
      f2 kf[11], o[8];
#pragma unroll
      for (int j = 0; j < 11; ++j) kf[j] = unp(kr[j]);
#pragma unroll
      for (int j = 0; j < 8; ++j) o[j] = (kf[j] * w3 + kf[j + 1] * w2 + kf[j + 2] * w1 + kf[j + 3] * w0) * unp(vv[j]);
#pragma unroll
      for (int e = 0; e < 2; ++e) {
        const u4 r = {pks(o[7][e], o[6][e]), pks(o[5][e], o[4][e]), pks(o[3][e], o[2][e]), pks(o[1][e], o[0][e])};
        *(u4*)(img + (2 * c2 + e) * 128 + 16 * ((7 - tc + c2) & 7)) = r;
      }
    }
    arrive(SY(C_B1));
    STAMP_AT(2);
    await<1>(SY(C_GO), 1);
    await<0>(SY(C_B1), 5);
    const rsrc ro = mkrs(ws + WS_KV + 80 * xcd * 4096 + 3968 - 128 * idx, 0x7fffffff);
#pragma unroll
    for (int j = 0; j < 2; ++j) {
      const int ci = t + 320 * j, ch = ci >> 3, k = ci & 7;
      const u4 v = *(const u4*)(img + ch * 128 + 16 * ((k + (ch >> 1)) & 7));
      st128<SC1>(v, ro, ch * 4096 + 16 * k, 0);
    }
    arrive(SY(C_B1D));
    __builtin_amdgcn_s_setprio(0);
    STAMP_AT(3);
    conv = true;
  } else {
    __builtin_amdgcn_s_setprio(3);
    if (w == 13) {
      unsigned epoch = ld32<SC1>(rf, FL_EPOCH, 0);
      __builtin_amdgcn_s_sleep(D_TAB);
      {
        const char* tp = (const char*)&TAB + 16 * l;
        PIN(tp);
#pragma unroll
        for (int f = 0; f < F_ALL; f += 3) {
          const GLB void* gp = (const GLB void*)(tp + 1024 * f);
          LDS void* lp = (LDS void*)(smem + L_TAB + 1024 * f);
          __builtin_amdgcn_global_load_lds(gp, lp, 16, 0, 0);
          __builtin_amdgcn_global_load_lds(gp, lp, 16, 1024, 0);
          __builtin_amdgcn_global_load_lds(gp, lp, 16, 2048, 0);
        }
      }
      asm volatile("s_waitcnt vmcnt(0)" : "+v"(sink), "+v"(epoch) :: "memory");
      *(unsigned*)(smem + SY(C_TAG)) = epoch * 0x9E3779B1u + 0x7F4A7C15u;
      arrive(SY(C_TAB));
    } else if (w == 14) {
      char* p = ws + WS_T1 + (2 * idx * T1P + 1408 * xcd) * 4;
      fill<6>(p, 5632, l); fill<6>(p + T1P * 4, 5632, l);
      drain();
      arrive(SY(C_PRE));
    } else {
      unsigned epoch = ld32<SC1>(rf, FL_EPOCH, 0);
      fill<10>(ws + WS_KV + (32 * xcd + idx) * 10240, 10240, l);
      drain();
      const unsigned tag = epoch * 0x9E3779B1u + 0x7F4A7C15u;
      await<1>(SY(C_PRE), 1);
      STAMP_AT(2);
      flag_round(rf, 0, tag, l, xcd);
      arrive(SY(C_GO));
    }
    __builtin_amdgcn_s_setprio(0);
    STAMP_AT(1);
    const int gi = w - 13;
    const int ch = 80 * xcd + 5 * (idx >> 1) + (gi == 2 ? 2 : gi + 3 * (idx & 1));
    const rsrc rk = mkrs(ssk + ch * 2048, 4096);
    u4 kc[4]; u2 kp[4];
#pragma unroll
    for (int j = 0; j < 4; ++j) { kc[j] = ld128(rk, 16 * l + 1024 * j, 0); kp[j] = ld64(rk, j ? 16 * l + 1024 * j - 8 : 16 * l - 8, 0); }
    char* base = smem + L_GRP + gi * GRP;
    const u4 z4 = {0u, 0u, 0u, 0u};
    if (l < 40) *(u4*)(base + (l < 8 ? (l >> 1) * KCS + 16 * (l & 1) : G_U + 4096 + 16 * (l - 8))) = z4;
#pragma unroll
    for (int j = 0; j < 4; ++j) {
      const unsigned o0 = __builtin_amdgcn_alignbit(kp[j][1], kp[j][0], 16), o1 = __builtin_amdgcn_alignbit(kc[j][0], kp[j][1], 16),
                     o2 = __builtin_amdgcn_alignbit(kc[j][1], kc[j][0], 16), o3 = __builtin_amdgcn_alignbit(kc[j][2], kc[j][1], 16),
                     o4 = __builtin_amdgcn_alignbit(kc[j][3], kc[j][2], 16);
      char* kp0 = base + 32 + 16 * l + 1024 * j;
      const u4 c1 = {o1, o2, o3, o4}, c2 = {kp[j][1], kc[j][0], kc[j][1], kc[j][2]}, c3 = {o0, o1, o2, o3};
      *(u4*)(kp0) = kc[j]; *(u4*)(kp0 + KCS) = c1; *(u4*)(kp0 + 2 * KCS) = c2; *(u4*)(kp0 + 3 * KCS) = c3;
    }
    arrive(SY(C_K + gi));
    STAMP_AT(3);
    conv = true;
  }

  if (conv) {
    const int gi = w < 4 ? 2 : (w - 8) >> 2, wl = w & 3, t = tid & 255;
    const int ch = 80 * xcd + 5 * (idx >> 1) + (gi == 2 ? 2 : gi + 3 * (idx & 1));
    char* base = smem + L_GRP + gi * GRP;
    char* ul = base + G_U;
    const rsrc ru = mkrs(ws + WS_KV + ch * 4096, 4096);
    float dv = blo(ssd[ch]);
    int ao = (3 - (i >> 2)) * KCS + 16 * g + 8 * (i & 3) + 8 + 128 * wl;
    int bo = G_U + 4064 - 3584 - 32 * i + 16 * g + 128 * wl;
    int wo = wl * 1024 + 16 * (4 * i + ((g + (i >> 1)) & 3));
    const int c = (t >> 1) & 15, r0 = (wl < 2 ? G_P : -16384) + 4096 * (t >> 5) + 64 * c;
    int p0 = r0 + 16 * ((2 * (t & 1) + (c >> 1)) & 3), p1 = r0 + 16 * ((2 * (t & 1) + 1 + (c >> 1)) & 3);
    const int mask = gi < 2 ? 0xff : ((idx & 1) ? 0x3b : 0xc4);
    int yo = ((mask >> (t >> 5)) & 1) ? 16 * t : 4096;
    PIN(dv); PIN(ao); PIN(bo); PIN(wo); PIN(p0); PIN(p1); PIN(yo);
    await<2>(SY(C_B1D), 5);
    if (w >= 8) __builtin_amdgcn_s_sleep(D_B2);
    u4 uv;
    SPIN_LOOP {
      CFENCE();
      uv = ld128<SC1>(ru, 16 * t, 0);
      if (__all(umax4(0u, uv) != ~0u)) break;
    }
    *(u4*)(ul + 16 * t) = uv;
    const int cg = SY(C_G + gi);
    arrive(cg);
    await<0>(SY(C_K + gi), 1);
    await<0>(SY(C_W), 3);
    await<0>(cg, 4);
    STAMP_AT(4);
    if (gi < 2) conv_part<0xff>(base, ao, bo, wo, cg);
    else if (idx & 1) conv_part<0x3b>(base, ao, bo, wo, cg);
    else conv_part<0xc4>(base, ao, bo, wo, cg);
    arrive(cg);
    const u4 kv = *(const u4*)(ul + 4080 - 16 * t);
    f4 s0 = {bhi(kv[3]), blo(kv[3]), bhi(kv[2]), blo(kv[2])}, s1 = {bhi(kv[1]), blo(kv[1]), bhi(kv[0]), blo(kv[0])};
    s0 *= dv; s1 *= dv;
    PIN(s0); PIN(s1);
    await<0>(SY(C_GO2), 1);
    await<0>(cg, 12);
#pragma unroll
    for (int ww = 0; ww < 4; ++ww) { s0 += *(const f4*)(base + p0 + ww * 1024); s1 += *(const f4*)(base + p1 + ww * 1024); }
    const u4 o = {pks(s0[0], s0[1]), pks(s0[2], s0[3]), pks(s1[0], s1[1]), pks(s1[2], s1[3])};
    st128<SC1>(o, mkrs(ws + WS_Y + ch * 4096, 4096), yo, 0);
    arrive(SY(C_B2D));
    STAMP_AT(5);
  }

  if (w < 8) {
    u4 C0 = FRAG(F_C0), S0 = FRAG(F_S0), QC = FRAG(F_QC), QS = FRAG(F_QS), QM = FRAG(F_QM);
    f4 tc[3], tn[3];
#pragma unroll
    for (int j = 0; j < 3; ++j) { tc[j] = FRAGF(F_TC + j); tn[j] = FRAGF(F_TS + j); PIN(tc[j]); PIN(tn[j]); }
    u4 N0 = S0 ^ SGN2;
    PIN(C0); PIN(S0); PIN(N0); PIN(QC); PIN(QS); PIN(QM);
    int zo = L_U + 2 * w * ZP + 40 * (8 * h + ((l >> 2) & 3)) + 8 * min(4 * ((l >> 4) & 1) + (l & 3), 4);
    int oo = n32 < 20 ? 64 * n32 + 16 * h : 4096;
    PIN(zo); PIN(oo);
    if (w < 6) {
      u4 m3[2];
      const float scale = 2.f / (2048.f * 640.f);
      const float sf = (float)(64 * (8 * q + (i >> 1)) + s2) * (1.f / 2048.f);
      const float ph = -0.25f * (float)(i & 1);
#pragma unroll
      for (int kk = 0; kk < 2; ++kk)
#pragma unroll
        for (int jj = 0; jj < 4; ++jj) {
          const float a = fmaf(sf, (float)(16 * kk + jj - 16 + 4 * g), ph);
          m3[kk][jj] = pk(fcos(a) * scale, fsin(a) * scale);
        }
      const rsrc rt = mkrs(ws + WS_T2 + s2 * NCOL * 4, NCOL * 4);
      int vo = 4 * (4 * g * 321 + (w < 5 ? 64 * w + 4 * i : 317));
      int wo = L_U + 4 * g * ZP + 128 * w + 8 * i;
      PIN(m3[0]); PIN(m3[1]); PIN(vo); PIN(wo);
      await<2>(SY(C_A2D), 3);
      __builtin_amdgcn_s_sleep(D_A3);
      u4 tv[8];
      SPIN_LOOP {
        CFENCE();
        unsigned m = 0;
#pragma unroll
        for (int j = 0; j < 8; ++j) tv[j] = ld128<SC1>(rt, vo + (j & 3) * 1284, (j >> 2) * 16 * 1284);
#pragma unroll
        for (int j = 0; j < 8; ++j) m = umax4(m, tv[j]);
        if (__all(m != ~0u)) break;
      }
      STAMP_AT(6);
      f4 acc[4];
#pragma unroll
      for (int t = 0; t < 4; ++t) {
        const u4 b0 = {tv[0][t], tv[1][t], tv[2][t], tv[3][t]}, b1 = {tv[4][t], tv[5][t], tv[6][t], tv[7][t]};
        const f4 z = {0.f, 0.f, 0.f, 0.f};
        acc[t] = mfma16(m3[1], b1, mfma16(m3[0], b0, z));
      }
#pragma unroll
      for (int r = 0; r < 4; ++r) {
        const u2 o = {pk(acc[0][r], acc[1][r]), pk(acc[2][r], acc[3][r])};
        *(u2*)(smem + wo + r * ZP) = o;
      }
      arrive(SY(C_A3));
    }
    await<1>(SY(C_A3), 6);
    {
      const char* zp = smem + zo;
      const u4 are = cat(trread(zp), trread(zp + 160)), aim = cat(trread(zp + ZP), trread(zp + ZP + 160));
      const float nyq = bhi(*(const unsigned*)(smem + L_U + 2 * w * ZP + 644));
      f16v vr = {}, vi = {};
      vr = mfma32(are, C0, vr); vi = mfma32(are, S0, vi);
      vr = mfma32(aim, N0, vr); vi = mfma32(aim, C0, vi);
      f16v y;
#pragma unroll
      for (int r = 0; r < 16; ++r) y[r] = (r & 1) ? -nyq : nyq;
      u4 br, bi, bm;
      twiddle<true>(vr, vi, tc, tn, br, bi, bm);
      y = mfma32(br, QC, y); y = mfma32(bi, QS, y); y = mfma32(bm, QM, y);
      const rsrc ro = mkrs(out + (64 * (8 * q + w) + s2) * 1280, 1280);
#pragma unroll
      for (int t = 0; t < 2; ++t) {
        unsigned a0 = pk(y[8 * t], y[8 * t + 1]), a1 = pk(y[8 * t + 2], y[8 * t + 3]);
        unsigned b0 = pk(y[8 * t + 4], y[8 * t + 5]), b1 = pk(y[8 * t + 6], y[8 * t + 7]);
        const auto r0 = __builtin_amdgcn_permlane32_swap(a0, b0, false, false);
        const auto r1 = __builtin_amdgcn_permlane32_swap(a1, b1, false, false);
        const u4 o = {r0[0], r1[0], r0[1], r1[1]};
        st128<WT_O>(o, ro, oo + 32 * t, 0);
      }
    }
    if (w == 4 && blockIdx.x == 0 && l == 0) st32<SC1>(*(const unsigned*)(smem + SY(C_TAG)), rf, FL_EPOCH, 0);
  } else if (w < 13) {
    const int t = tid - 512;
    char* img = smem + L_IMG;
    u4 yv[2], qv[2];
    const rsrc ry = mkrs(ws + WS_Y + 80 * xcd * 4096 + 128 * idx, 0x7fffffff), rq = mkrs(qin + 64 * idx * 640 + 80 * xcd, 0x7fffffff);
    const int gi = t >> 4, blk = gi / 10, c8 = gi % 10;
#pragma unroll
    for (int j = 0; j < 2; ++j) qv[j] = ld128(rq, (16 * blk + i) * 1280 + 16 * c8, j * 32 * 1280);
    f2 qf[2][4];
#pragma unroll
    for (int j = 0; j < 2; ++j)
#pragma unroll
      for (int e = 0; e < 4; ++e) { qf[j][e] = unp(qv[j][e]); PIN(qf[j][e]); }
    int yi = (t >> 3) * 4096 + 16 * (t & 7), wo = L_IMG + (t >> 3) * 144 + 16 * (t & 7);
    int po = L_IMG + (8 * c8 + (i >> 2)) * 144 + 32 * blk + 8 * (i & 3), oo = (16 * blk + i) * 2560 + 16 * c8;
    PIN(yi); PIN(wo); PIN(po); PIN(oo);
    await<2>(SY(C_B2D), 12);
    __builtin_amdgcn_s_sleep(D_B3);
    SPIN_LOOP {
      CFENCE();
#pragma unroll
      for (int j = 0; j < 2; ++j) yv[j] = ld128<SC1>(ry, yi, j * 40 * 4096);
      if (__all(umax4(umax4(0u, yv[0]), yv[1]) != ~0u)) break;
    }
    STAMP_AT(6);
#pragma unroll
    for (int j = 0; j < 2; ++j) *(u4*)(smem + wo + 40 * 144 * j) = yv[j];
    arrive(SY(C_B1));
    await<0>(SY(C_B1), 10);
    const rsrc ro = mkrs(out + 64 * idx * 1280 + 640 + 80 * xcd, 0x7fffffff);
#pragma unroll
    for (int j = 0; j < 2; ++j) {
      const char* p = smem + po + 64 * j;
      const u4 y = cat(trread(p), trread(p + 4 * 144));
      u4 o;
#pragma unroll
      for (int e = 0; e < 4; ++e) { const f2 v = unp(y[e]) * qf[j][e]; o[e] = pk(v[0], v[1]); }
      st128<WT_O>(o, ro, oo, j * 32 * 2560);
    }
  }
  STAMP_AT(7);
}

extern "C" void kernel_launch(void* const* d_in, const int* in_sizes, int n_in, void* d_out, int out_size, void* d_ws,
                              size_t ws_size, hipStream_t stream) {
#define SA
  gfh3<<<256, 1024, 0, stream>>>((const uint16_t*)d_in[0], (const uint16_t*)d_in[1], (const uint16_t*)d_in[2], (const uint16_t*)d_in[3],
                                 (const uint16_t*)d_in[4], (const uint16_t*)d_in[5], (const uint16_t*)d_in[6], (const uint16_t*)d_in[7],
                                 (const uint16_t*)d_in[8], (uint16_t*)d_out, (char*)d_ws SA);
}
```

```cpp
#include <hip/hip_runtime.h>
#include <stdint.h>
#include <utility>

typedef __attribute__((ext_vector_type(2))) __bf16 bf2;
typedef __attribute__((ext_vector_type(8))) __bf16 bf8;
typedef __attribute__((ext_vector_type(2))) float f2;
typedef __attribute__((ext_vector_type(4))) float f4;
typedef __attribute__((ext_vector_type(16))) float f16v;
typedef __attribute__((ext_vector_type(2))) unsigned u2;
typedef __attribute__((ext_vector_type(4))) unsigned u4;
typedef __attribute__((ext_vector_type(4))) short s4;
typedef __amdgpu_buffer_rsrc_t rsrc;
#define DI __device__ __forceinline__
#define LDS __attribute__((address_space(3)))
#define GLB __attribute__((address_space(1)))
#define PIN(x) asm volatile("" : "+v"(x))
#define SCHED() __builtin_amdgcn_sched_barrier(0)
#define BAR() asm volatile("s_barrier" ::: "memory")
#define LBAR() asm volatile("s_waitcnt lgkmcnt(0)\n\ts_barrier" ::: "memory")
#define CFENCE() asm volatile("" ::: "memory")
#define SPIN_LOOP _Pragma("unroll 1") for (unsigned spin = 0; spin < SPIN_MAX; ++spin)

#define XA 500
#define XB 420
#define X2A 800
#define X2B 850
constexpr int D_X2 = 10, D_RETRY = 4;
constexpr int NT = 2;
constexpr int SC1 = 16;
constexpr int NCOL = 32 * 321;
constexpr int SLOT = 352;
constexpr int TP = 32 * SLOT + 32;
constexpr int CP = 4096 + 128;
constexpr unsigned SGN2 = 0x80008000u;
constexpr unsigned SPIN_MAX = 1u << 18;

constexpr int WS_T = 0, WS_KV = 64 * TP * 4, WS_FLAG = WS_KV + 768 * CP, FL_A1 = 0, FL_B1 = 8 * 1024, FL_EPOCH = 9 * 1024;

enum { F_C0, F_S0, F_PC, F_PS, F_MR, F_QC, F_QS, F_QM, F_TC, F_TS = F_TC + 3, F_AR = F_TS + 3, F_M3 = F_AR + 4, F_ALL = F_M3 + 2 };

DI unsigned pk(float a, float b) { f2 v = {a, b}; return __builtin_bit_cast(unsigned, __builtin_convertvector(v, bf2)); }
DI float blo(unsigned v) { return __uint_as_float(v << 16); }
DI float bhi(unsigned v) { return __uint_as_float(v & 0xffff0000u); }
DI f2 unp(unsigned v) { f2 r = {blo(v), bhi(v)}; return r; }
DI float fcos(float r) { return __builtin_amdgcn_cosf(r); }
DI float fsin(float r) { return __builtin_amdgcn_sinf(r); }
DI f4 mfma16(u4 a, u4 b, f4 c) { return __builtin_amdgcn_mfma_f32_16x16x32_bf16(__builtin_bit_cast(bf8, a), __builtin_bit_cast(bf8, b), c, 0, 0, 0); }
DI f16v mfma32(u4 a, u4 b, f16v c) { return __builtin_amdgcn_mfma_f32_32x32x16_bf16(__builtin_bit_cast(bf8, a), __builtin_bit_cast(bf8, b), c, 0, 0, 0); }
DI u2 trread(const char* p) { return __builtin_bit_cast(u2, __builtin_amdgcn_ds_read_tr16_b64_v4i16((LDS s4*)p)); }
DI u4 cat(u2 a, u2 b) { u4 r = {a[0], a[1], b[0], b[1]}; return r; }
DI rsrc mkrs(const void* p, unsigned bytes) { return __builtin_amdgcn_make_buffer_rsrc((void*)p, 0, bytes, 0x00020000); }
template <int AUX = 0> DI u4 ld128(rsrc r, int v, int s) { return __builtin_bit_cast(u4, __builtin_amdgcn_raw_buffer_load_b128(r, v, s, AUX)); }
template <int AUX = 0> DI u2 ld64(rsrc r, int v, int s) { return __builtin_bit_cast(u2, __builtin_amdgcn_raw_buffer_load_b64(r, v, s, AUX)); }
template <int AUX = 0> DI unsigned ld32(rsrc r, int v, int s) { return __builtin_amdgcn_raw_buffer_load_b32(r, v, s, AUX); }
template <int AUX> DI void st128(u4 x, rsrc r, int v) { __builtin_amdgcn_raw_buffer_store_b128(x, r, v, 0, AUX); }
template <int AUX> DI void st32(unsigned x, rsrc r, int v, int s) { __builtin_amdgcn_raw_buffer_store_b32(x, r, v, s, AUX); }
DI unsigned and4(unsigned m, u4 a) { return m & a[0] & a[1] & a[2] & a[3]; }
DI void drain() { asm volatile("s_waitcnt vmcnt(0)" ::: "memory"); }
template <int H> DI f2 half(const f4& a) { return __builtin_shufflevector(a, a, 2 * H, 2 * H + 1); }
template <int H> DI f2 half(const f16v& a) { return __builtin_shufflevector(a, a, 2 * H, 2 * H + 1); }
template <int SA, int SB> DI f2 mad(f2 a, f2 b, f2 c, f2 d) {
  const f2 t = c * d;
  return __builtin_elementwise_fma(SA > 0 ? a : -a, b, SB > 0 ? t : -t);
}

DI void arrive(int off) {
  asm volatile("s_mov_b64 exec, 1\n\tds_add_u32 %0, %1\n\ts_mov_b64 exec, -1" :: "v"(off), "v"(1u) : "memory");
}
template <int SLEEP> DI void await(int off, int n) {
  unsigned v = 0, s;
  asm volatile("1:\n\tds_read_b32 %0, %2\n\ts_waitcnt lgkmcnt(0)\n\tv_readfirstlane_b32 %1, %0\n\ts_cmp_ge_u32 %1, %3\n\t"
               "s_cbranch_scc1 2f\n\ts_sleep %4\n\ts_branch 1b\n2:" : "+&v"(v), "=&s"(s) : "v"(off), "i"(n), "i"(SLEEP) : "memory", "scc");
}

constexpr int KCS = 4160;
constexpr int G_P = 4 * KCS, G_U = G_P + 16384;
constexpr int GRP = G_U + 4608;
constexpr int ZP = 768;
constexpr int L_SYNC = 0, L_TAB = 128, L_U = L_TAB + F_ALL * 1024, L_IMG = L_U + 18432, L_GRP = L_IMG + 80 * 144, L_END = L_GRP + 3 * GRP;
enum { C_GOA, C_GOB, C_A1, C_A1D, C_B1, C_B1S, C_B1D, C_K, C_G = C_K + 3, C_A2D = C_G + 3, C_A3, C_B2D, C_PUBA, C_XI, C_Q, C_TAG = 31 };
#define SY(c) (L_SYNC + 4 * (c))

#define OPAQUE8(a) asm("" : "+v"((a)[0]), "+v"((a)[1]), "+v"((a)[2]), "+v"((a)[3]), "+v"((a)[4]), "+v"((a)[5]), "+v"((a)[6]), "+v"((a)[7]))
template <typename F> DI void dft64(const u4 (&ar)[4], const u4 (&ai)[4], const u4 (&b)[4], F f) {
  f4 er[4], ei[4];
  const f4 z = {0.f, 0.f, 0.f, 0.f};
#pragma unroll
  for (int c = 0; c < 4; ++c) { const int cc = (c >> 1) | ((c & 1) << 1); er[cc] = mfma16(ar[cc], b[cc], z); ei[cc] = mfma16(ai[cc], b[cc], z); }
#pragma unroll
  for (int rr = 0; rr < 4; ++rr) {
    const int r = (rr >> 1) | ((rr & 1) << 1);
    float t[8] = {er[0][r] + er[2][r], ei[0][r] + ei[2][r], er[0][r] - er[2][r], ei[0][r] - ei[2][r],
                  er[1][r] + er[3][r], ei[1][r] + ei[3][r], er[1][r] - er[3][r], ei[1][r] - ei[3][r]};
    OPAQUE8(t);
    float x[8] = {t[0] + t[4], t[1] + t[5], t[2] + t[7], t[3] - t[6], t[0] - t[4], t[1] - t[5], t[2] - t[7], t[3] + t[6]};
    OPAQUE8(x);
    f(r, x);
  }
}

template <bool INV, int J> DI void tw1(const f16v& vr, const f16v& vi, const f4 (&tc)[3], const f4 (&tn)[3], unsigned& pr, unsigned& pi) {
  const f2 a = half<J>(vr), b = half<J>(vi), c = half<J & 1>(tc[J >> 1]), s = half<J & 1>(tn[J >> 1]);
  const f2 re = mad<1, INV ? -1 : 1>(a, c, b, s), im = mad<1, INV ? 1 : -1>(b, c, a, s);
  pr = pk(re[0], re[1]); pi = pk(im[0], im[1]);
}
template <bool INV> DI void twiddle(const f16v& vr, const f16v& vi, const f4 (&tc)[3], const f4 (&tn)[3], u4& br, u4& bi, u4& bm) {
  unsigned pr[6], pi[6];
  tw1<INV, 0>(vr, vi, tc, tn, pr[0], pi[0]); tw1<INV, 1>(vr, vi, tc, tn, pr[1], pi[1]); tw1<INV, 2>(vr, vi, tc, tn, pr[2], pi[2]);
  tw1<INV, 3>(vr, vi, tc, tn, pr[3], pi[3]); tw1<INV, 4>(vr, vi, tc, tn, pr[4], pi[4]); tw1<INV, 5>(vr, vi, tc, tn, pr[5], pi[5]);
  br = (u4){pr[0], pr[1], pr[2], pr[3]}; bi = (u4){pi[0], pi[1], pi[2], pi[3]}; bm = (u4){pr[4], pr[5], pi[4], pi[5]};
}

struct ConvList { int n, a[36], b[36]; };
constexpr ConvList conv_list(int mask) {
  ConvList c{};
  for (int s = 0; s < 8; ++s) {
    for (int e = 0; e <= s; ++e) if (s + e < 8 && ((mask >> (s + e)) & 1)) { c.a[c.n] = s; c.b[c.n] = e; ++c.n; }
    for (int e = 0; e < s; ++e) if (s + e < 8 && ((mask >> (s + e)) & 1)) { c.a[c.n] = e; c.b[c.n] = s; ++c.n; }
  }
  return c;
}
constexpr int conv_rd(int n) {
  constexpr int RB[5] = {0, 4, 10, 14, 17};
  for (int f = 0; f < 5; ++f) if (n == RB[f]) return 3 * (3 + f);
  constexpr int RF[8] = {21, 24, 27, 32, 36, 39, 42, 45};
  for (int f = 0; f < 8; ++f) if (n >= RF[f] && n < RF[f] + 3) return 3 * (8 + f) + n - RF[f];
  return -1;
}
constexpr int conv_wr(int n) {
  constexpr int WF[7] = {49, 51, 53, 57, 63, 68, 71};
  for (int j = 0; j < 7; ++j) if (n == WF[j]) return j;
  return -1;
}
template <typename F, int... I> DI void static_for_(F f, std::integer_sequence<int, I...>) { (f(std::integral_constant<int, I>{}), ...); }
template <int N, typename F> DI void static_for(F f) { static_for_(f, std::make_integer_sequence<int, N>{}); }
template <int MASK> DI void conv_part(char* base, int ao, int bo, int wo, int cg, const u4 (&a0)[8]) {
  constexpr int NB = (MASK & 0x80) ? 8 : 6;
  constexpr ConvList L = conv_list(MASK);
  constexpr bool FULL = MASK == 0xff;
  constexpr int PRO = FULL ? 3 : 4;
  f4 acc[8];
#pragma unroll
  for (int j = 0; j < 8; ++j) acc[j] = (f4){0.f, 0.f, 0.f, 0.f};
  u4 b[2 * NB]; u2 al[NB], ah[NB];
  auto rd = [&](int f, int part) {
    const int rr = f / NB, e = f % NB;
    if (part == 0) b[f] = *(const u4*)(base + bo + 512 * (7 - e) + 64 * rr);
    if (part == 1) al[e] = trread(base + ao + 512 * e + 64);
    if (part == 2) ah[e] = trread(base + ao + 512 * e + 64 + 8);
  };
  auto wr = [&](int j) { if ((MASK >> j) & 1) *(f4*)(base + wo + (j < 4 ? G_P + 4096 * j : 4096 * (j - 4))) = acc[j]; };
#pragma unroll
  for (int f = 0; f < PRO; ++f) { rd(f, 0); SCHED(); }
  constexpr int R1 = 9;
  constexpr int LASTRD = FULL ? 47 : R1 + (3 * NB - 1) / 2;
  unsigned seen = 0;
  static_for<2 * L.n>([&](auto nc) {
    constexpr int n = decltype(nc)::value, o = n < L.n ? 0 : NB, fa = L.a[n % L.n], fb = L.b[n % L.n];
    acc[fa + fb] = mfma16(n < L.n ? a0[fa] : cat(al[fa], ah[fa]), b[o + fb], acc[fa + fb]);
    SCHED();
    if constexpr (FULL) {
      constexpr int f = conv_rd(n), j = conv_wr(n);
      if constexpr (f >= 0) rd(f / 3, f % 3);
      if constexpr (n == 58) seen = *(volatile LDS unsigned*)(size_t)cg;
      if constexpr (j == 4) { if (__builtin_amdgcn_readfirstlane(seen) < 8) await<0>(cg, 8); }
      if constexpr (j >= 0) wr(j);
    } else {
      if constexpr (n < NB - PRO) rd(PRO + n, 0);
      constexpr int k = 2 * (n - R1);
      if constexpr (k >= 0 && k < 3 * NB) { rd(NB + k / 3, k % 3); rd(NB + (k + 1) / 3, (k + 1) % 3); }
    }
    if constexpr (n == LASTRD + 1) arrive(cg);
    SCHED();
  });
  if (FULL) wr(7);
  else {
    await<0>(cg, 8);
#pragma unroll
    for (int j = 0; j < 8; ++j) wr(j);
  }
}

DI void glds16(const void* src, int dst) {
  unsigned keep;
  asm volatile("s_mov_b32 %0, m0\n\ts_mov_b32 m0, %2\n\ts_nop 0\n\tglobal_load_lds_dwordx4 %1, off nt\n\ts_mov_b32 m0, %0"
               : "=&s"(keep) : "v"(src), "s"(dst) : "memory");
}
DI unsigned now() { return (unsigned)__builtin_amdgcn_s_memrealtime(); }
DI void pace(unsigned t0, int x, int p) {
  const unsigned t1 = now();
  while ((int)(now() - t0) < x || (int)(now() - t1) < p) __builtin_amdgcn_s_sleep(1);
}
template <bool WIDE> DI void gather(rsrc rf, int off, unsigned tag) {
  SPIN_LOOP {
    CFENCE();
    bool ok;
    if (WIDE) { const u4 f = ld128<SC1>(rf, off, 0); ok = f[0] == tag && f[1] == tag && f[2] == tag && f[3] == tag; }
    else ok = ld32<SC1>(rf, off, 0) == tag;
    if (__all(ok)) break;
    __builtin_amdgcn_s_sleep(2);
  }
}

DI int fresh_lane() {
  unsigned z = 0;
  PIN(z);
  return __builtin_amdgcn_mbcnt_hi(~0u, __builtin_amdgcn_mbcnt_lo(~0u, z));
}
#define LANES() const int l = fresh_lane(), tid = 64 * w + l, i = l & 15, g = l >> 4, n32 = l & 31, h = l >> 5; char* tab = smem + L_TAB + 16 * l; \
  (void)tid; (void)i; (void)g; (void)n32; (void)h; (void)tab

extern "C" __global__ __launch_bounds__(1024, 4) void gfh3(const uint16_t* __restrict__ x, const uint16_t* __restrict__ wr, const uint16_t* __restrict__ wi,
                                                        const uint16_t* __restrict__ qin, const uint16_t* __restrict__ kin, const uint16_t* __restrict__ vin,
                                                        const uint16_t* __restrict__ shk, const uint16_t* __restrict__ ssk, const uint16_t* __restrict__ ssd,
                                                        uint16_t* __restrict__ out, char* ws) {
  __shared__ __attribute__((aligned(16))) char smem[L_END];
  const int l = threadIdx.x & 63;
  const int wv0 = (__builtin_amdgcn_readfirstlane(threadIdx.x >> 6) + 8) & 15;
  const int w = wv0 >= 12 ? wv0 ^ 1 : wv0;
  const int tid = 64 * w + l;
  const int xcd = blockIdx.x & 7, idx = blockIdx.x >> 3;
  const int q = idx & 3, s2 = xcd * 8 + (idx >> 2);
  const int rb = 4 * xcd + (idx >> 3), cgp = idx & 7;
  const int i = l & 15, g = l >> 4, n32 = l & 31, h = l >> 5;
  char* tab = smem + L_TAB + 16 * l;
#define FRAG(f) (*(u4*)(tab + 1024 * (f)))
#define FRAGF(f) (*(f4*)(tab + 1024 * (f)))
  const unsigned tstart = now();
  unsigned sink = 0;
  if (w == 8) {
    unsigned long long pc; int rel, len;
    asm volatile("s_getpc_b64 %0\n\ts_mov_b32 %1, gfh3@rel32@lo+4\n\ts_mov_b32 %2, gfh3_code_end-gfh3" : "=s"(pc), "=s"(rel), "=s"(len));
    const char* p = (const char*)(pc + (long long)rel) + min(128 * (8 * l + (idx & 7)), len);
    asm volatile("global_load_dword %0, %1, off" : "+v"(sink) : "v"(p) : "memory");
  }
  asm volatile("" :: "s"(x), "s"(wr), "s"(wi), "s"(qin), "s"(kin), "s"(vin), "s"(shk), "s"(ssk), "s"(ssd), "s"(out), "s"(ws));
  const rsrc rf = mkrs(ws + WS_FLAG, FL_EPOCH + 4);
  const int gi = w < 8 ? 2 : (w - 8) >> 2;
  bool conv = false;

  if (__builtin_expect((unsigned)(w - 8) < 5u, 1)) {
    const int t = tid - 512, c2 = t % 40, tc = t / 40;
    unsigned kr[11], vv[8], epoch;
    u4 wk;
    {
      const rsrc rk = mkrs(kin, 2048 * 1280), rv = mkrs(vin, 2048 * 1280), rs = mkrs(shk, 640 * 8);
      const int vo = (64 * rb + 8 * tc) * 1280 + 160 * cgp + 4 * c2;
      const int vh = (rb | tc) ? vo - 3 * 1280 : 2048 * 1280;
      epoch = ld32<SC1>(rf, w == 8 ? FL_EPOCH : FL_EPOCH + 4, 0);
      SCHED();
#pragma unroll
      for (int j = 0; j < 8; ++j) { kr[3 + j] = ld32<NT>(rk, vo, j * 1280); vv[j] = ld32<NT>(rv, vo, j * 1280); }
      wk = ld128(rs, 640 * cgp + 16 * c2, 0);
#pragma unroll
      for (int j = 0; j < 3; ++j) kr[j] = ld32<NT>(rk, vh + j * 1280, 0);
      SCHED();
    }
    const rsrc ro = mkrs(ws + WS_KV + 96 * cgp * CP + 3968 - 128 * rb, 96 * CP - 4096);
    int so = (t >> 3) * CP + 16 * (t & 7), ri[3];
#pragma unroll
    for (int j = 0; j < 3; ++j) {
      const int r = min((t >> 3) + 40 * j, 95), ch = r - (r + 3) / 6;
      ri[j] = L_IMG + ch * 128 + 16 * (((t & 7) + (ch >> 1)) & 7);
      PIN(ri[j]);
    }
    int so2 = t < 128 ? so + 80 * CP : 96 * CP;
    int wi0 = L_IMG + 2 * c2 * 128 + 16 * ((7 - tc + c2) & 7);
    PIN(so); PIN(so2); PIN(wi0);
    SCHED();
    asm volatile("" : "+v"(sink), "+v"(epoch));
    if (w == 8) {
      *(unsigned*)(smem + SY(C_TAG)) = epoch * 0x9E3779B1u + 0x7F4A7C15u;
    }
    LBAR();
    __builtin_amdgcn_s_setprio(3);
    {
      const f2 w0 = {blo(wk[0]), blo(wk[2])}, w1 = {bhi(wk[0]), bhi(wk[2])}, w2 = {blo(wk[1]), blo(wk[3])}, w3 = {bhi(wk[1]), bhi(wk[3])};
      f2 kf[11], o[8];
#pragma unroll
      for (int j = 0; j < 11; ++j) kf[j] = unp(kr[j]);
#pragma unroll
      for (int j = 0; j < 8; ++j) o[j] = (kf[j] * w3 + kf[j + 1] * w2 + kf[j + 2] * w1 + kf[j + 3] * w0) * unp(vv[j]);
#pragma unroll
      for (int e = 0; e < 2; ++e) {
        const u4 r = {pk(o[7][e], o[6][e]), pk(o[5][e], o[4][e]), pk(o[3][e], o[2][e]), pk(o[1][e], o[0][e])};
        *(u4*)(smem + wi0 + e * 128) = r & ~1u;
      }
    }
    arrive(SY(C_B1));
    await<0>(SY(C_B1), 5);
#pragma unroll
    for (int j = 0; j < 3; ++j) st128<SC1>(*(const u4*)(smem + ri[j]), ro, j < 2 ? so + j * 40 * CP : so2);
    arrive(SY(C_B1S));
    conv = true;
  } else if (__builtin_expect(w < 5, 1)) {
    u4 xv[8];
    const rsrc rx = mkrs(x + s2 * 640 + 128 * w, 0x7fffffff);
    const int vx = g * (512 * 1280) + 16 * i;
#pragma unroll
    for (int j = 0; j < 2; ++j) xv[j] = ld128(rx, vx, (j + 2 * q) * 64 * 1280);
    SCHED();
    u4 m1;
    {
      const int wv = i >> 1;
      const float kf = (wv < 4 ? (float)(4 * q + wv + 1) : 0.f) * (1.f / 2048.f);
      const float ph = 0.25f * (float)(i & 1);
#pragma unroll
      for (int jj = 0; jj < 4; ++jj) {
        const float sb = (float)(512 * g + s2 + 128 * ((jj + q) & 3));
        m1[jj] = pk(fcos(fmaf(kf, sb, ph)), fcos(fmaf(kf, sb + 64.f, ph)));
      }
    }
    const int ks1 = w < 4 ? 4 * q + w + 1 : 0;
    const rsrc rt = mkrs(ws + WS_T + s2 * TP * 4, 32 * SLOT * 4);
    const int cm = (16 - ks1) * SLOT + (ks1 ? 0 : TP), cd = (ks1 + 16) * SLOT;
    int so = 4 * (h ? cm + 63 - n32 : cd + n32);
    int sx = 4 * ((h ? cd + 320 + n32 : cm + 31 - n32) + (n32 < 16 ? 0 : TP));
    int upo = L_U + 2 * w * 1280 + 40 * (8 * h + ((l >> 2) & 3)) + 8 * min(4 * ((l >> 4) & 1) + (l & 3), 4);
    int wro = L_U + 4 * g * 1280 + 256 * w + 16 * i;
    PIN(m1); PIN(so); PIN(sx); PIN(upo); PIN(wro);
    BAR();
#pragma unroll
    for (int j = 2; j < 8; ++j) xv[j] = ld128(rx, vx, ((j + 2 * q) & 7) * 64 * 1280);
    arrive(SY(C_XI));
    u4 C0 = FRAG(F_C0), S0 = FRAG(F_S0);
    f4 tc[3], tn[3];
#pragma unroll
    for (int j = 0; j < 3; ++j) { tc[j] = FRAGF(F_TC + j); PIN(tc[j]); }
    const unsigned sg = (n32 & 1) ? SGN2 : 0u;
    u4 N0 = S0 ^ SGN2, C1 = C0 ^ sg, S1 = S0 ^ sg, N1 = N0 ^ sg;
    PIN(C0); PIN(S0); PIN(N0); PIN(C1); PIN(S1); PIN(N1);
    SCHED();
    __builtin_amdgcn_s_setprio(3);
    {
      f4 acc[8];
      u4 b[8];
#pragma unroll
      for (int jj = 0; jj < 4; ++jj) {
#pragma unroll
        for (int t = 0; t < 8; ++t) {
          b[t][jj] = __builtin_amdgcn_perm(xv[2 * jj + 1][t >> 1], xv[2 * jj][t >> 1], (t & 1) ? 0x07060302u : 0x05040100u);
          const f4 z = {0.f, 0.f, 0.f, 0.f};
          if (jj == 3) acc[t] = mfma16(m1, b[t], z);
        }
        SCHED();
      }
      if (g < 3) {
#pragma unroll
        for (int r = 0; r < 4; ++r) {
          u4 o = {pk(acc[0][r], acc[1][r]), pk(acc[2][r], acc[3][r]), pk(acc[4][r], acc[5][r]), pk(acc[6][r], acc[7][r])};
          *(u4*)(smem + wro + r * 1280) = o;
        }
      }
    }
    arrive(SY(C_A1));
    if (w < 4 || q == 0) {
      const u4 PC = FRAG(F_PC), PS = FRAG(F_PS), MR = FRAG(F_MR);
#pragma unroll
      for (int j = 0; j < 3; ++j) tn[j] = FRAGF(F_TS + j);
      await<0>(SY(C_A1), 5);
      const char* up = smem + upo;
      u4 are[2], aim[2];
#pragma unroll
      for (int kk = 0; kk < 2; ++kk) {
        are[kk] = cat(trread(up + 640 * kk), trread(up + 640 * kk + 160));
        aim[kk] = cat(trread(up + 1280 + 640 * kk), trread(up + 1280 + 640 * kk + 160));
      }
      f16v vr = {}, vi = {};
      vr = mfma32(are[0], C0, vr); vi = mfma32(are[0], N0, vi);
      vr = mfma32(aim[0], S0, vr); vi = mfma32(aim[0], C0, vi);
      vr = mfma32(are[1], C1, vr); vi = mfma32(are[1], N1, vi);
      vr = mfma32(aim[1], S1, vr); vi = mfma32(aim[1], C1, vi);
      const u4 PN = PS ^ SGN2, MI = {MR[2] ^ SGN2, MR[3] ^ SGN2, MR[0], MR[1]};
      u4 br, bi, bm;
      twiddle<false>(vr, vi, tc, tn, br, bi, bm);
      f16v xr = {}, xi = {};
      xr = mfma32(PC, br, xr); xi = mfma32(PN, br, xi);
      xr = mfma32(PS, bi, xr); xi = mfma32(PC, bi, xi);
      xr = mfma32(MR, bm, xr); xi = mfma32(MI, bm, xi);
      const unsigned cj = h ? 0x80000000u : 0u;
      unsigned o[10];
#pragma unroll
      for (int r = 0; r < 10; ++r) { o[r] = (pk(xr[r], xi[r]) ^ cj) & ~1u; st32<SC1>(o[r], rt, so + 128 * r, 0); }
      st32<SC1>((h ? o[9] : o[0]) ^ 0x80000000u, rt, sx, 0);
    }
    if (w == 4) {
      LANES();
      drain();
      arrive(SY(C_A1D));
      const unsigned tag = *(const unsigned*)(smem + SY(C_TAG));
      await<1>(SY(C_A1D), 5);
      if (l < 8) st32<SC1>(tag, rf, FL_A1 + 1024 * l + 4 * blockIdx.x, 0);
      arrive(SY(C_PUBA));
      const char* qp[10];
#pragma unroll
      for (int j = 0; j < 10; ++j) {
        const int gq = (4 * j + g) % 20, blk = gq / 10 + 2 * (j / 5), c8 = gq % 10;
        qp[j] = (const char*)(qin + (64 * rb + 63 - i - 16 * blk) * 640 + 80 * cgp + 8 * c8);
        PIN(qp[j]);
      }
      await<1>(SY(C_B1D), 5);
      pace(tstart, XA, 25);
      gather<true>(rf, FL_A1 + 1024 * xcd + 16 * l, tag);
      arrive(SY(C_GOA));
#pragma unroll
      for (int j = 0; j < 10; ++j) glds16(qp[j], L_IMG + 1024 * j);
      drain();
      arrive(SY(C_Q));
    }
    conv = w < 4;
  } else if (w < 8) {
    const bool act = idx < 28;
    const int slot = 4 * xcd + idx / 7, tile = 3 * (idx % 7) + (w - 5);
    if (w < 7) {
      const int ro = (n32 & 3) + 4 * (n32 >> 3);
      const float a20 = w == 5 ? (float)((n32 & 4) ? 19 - ro : ro) * 0.05f : (float)n32 * -0.05f;
      u4 c, s, m;
#pragma unroll
      for (int jj = 0; jj < 4; ++jj) {
        const float a = a20 * (float)(8 * (jj >> 1) + 4 * h + 2 * (jj & 1)), b = a + a20;
        c[jj] = pk(fcos(a), fcos(b)); s[jj] = pk(fsin(a), fsin(b));
      }
#pragma unroll
      for (int jj = 0; jj < 2; ++jj) {
        const float a = a20 * (float)(16 + 2 * jj), b = a + a20;
        m[jj] = h ? 0u : pk(fcos(a), fcos(b)); m[2 + jj] = h ? 0u : pk(fsin(a), fsin(b));
      }
      char* tp = tab + (w - 5) * 3072;
      *(u4*)(tp + 1024 * F_PC) = c; *(u4*)(tp + 1024 * F_PS) = s; *(u4*)(tp + 1024 * F_MR) = m;
    } else {
      const float a64 = (float)i * (1.f / 64.f);
#pragma unroll
      for (int c = 0; c < 4; ++c) {
        u4 v;
#pragma unroll
        for (int u = 0; u < 4; ++u) { const float a = a64 * (float)(16 * u + c + 4 * g); v[u] = pk(fcos(a), fsin(a)); }
        FRAG(F_AR + c) = v;
      }
    }
    LBAR();
    u4 ar[4], ai[4];
#pragma unroll
    for (int c = 0; c < 4; ++c) {
      ar[c] = FRAG(F_AR + c);
#pragma unroll
      for (int u = 0; u < 4; ++u) ai[c][u] = __builtin_amdgcn_alignbit(ar[c][u], ar[c][u], 16) ^ 0x8000u;
      PIN(ar[c]); PIN(ai[c]);
    }
    const rsrc rt = mkrs(ws + WS_T, 64 * TP * 4);
    int vo = 4 * (4 * g * TP + slot * SLOT + 16 * tile + (slot < 16 ? 16 : 0) + i);
    const rsrc ra = mkrs(wr, act ? 128 * NCOL : 0), ri = mkrs(wi, act ? 128 * NCOL : 0);
    const int w0 = (slot - 16) * 321 + 16 * tile - (slot < 16 ? 15 : 0);
    int wo[3];
#pragma unroll
    for (int j = 0; j < 3; ++j) {
      const int c = l + 64 * j;
      wo[j] = 2 * ((w0 & ~7) + (c / 3) * NCOL) + 16 * (c % 3);
      if (j == 0) wo[j] += (wo[j] < 0) ? 128 * NCOL : 0;
      PIN(wo[j]);
    }
    PIN(vo);
    if (w < 7) {
      int o = 2 * (1284 * xcd - 16 * 321 - 16 + (2 * idx + h) * NCOL) + 128 * n32;
      o += (o < 0) ? 128 * NCOL : 0;
      await<1>(SY(C_B1S), 5);
      const unsigned t0 = ld32(mkrs(w == 5 ? wr : wi, 128 * NCOL), n32 < 22 ? o : 128 * NCOL, 0);
      asm volatile("" :: "v"(t0));
    }
    await<1>(SY(C_PUBA), 1);
    __builtin_amdgcn_s_setprio(3);
    u4 wv[6];
#pragma unroll
    for (int j = 0; j < 3; ++j) { wv[j] = ld128(ra, wo[j], 0); wv[3 + j] = ld128(ri, wo[j], 0); }
    char* wsc = smem + L_U + (w - 5) * 6144;
#pragma unroll
    for (int j = 0; j < 6; ++j) *(u4*)(wsc + 1024 * j + 16 * l) = wv[j];
    f2 wa[4][2], wb[4][2];
    const int kd = 16 * tile - (slot < 16 ? 15 : 0) + i;
    const float wsc2 = (kd == 0 || kd == 320) ? 0.5f : 1.f;
#pragma unroll
    for (int u = 0; u < 4; ++u) {
#pragma unroll
      for (int r = 0; r < 4; ++r) {
        const char* p = wsc + 192 * g + 2 * (i + (w0 & 7)) + 768 * u + 48 * r;
        wa[u][r >> 1][r & 1] = blo(*(const uint16_t*)p); wb[u][r >> 1][r & 1] = blo(*(const uint16_t*)(p + 3072));
      }
      PIN(wa[u][0]); PIN(wb[u][0]); PIN(wa[u][1]); PIN(wb[u][1]);
    }
    if (tile == 0 || tile == 20) {
#pragma unroll
      for (int e = 0; e < 16; ++e) {
        float ta = wa[e >> 2][(e >> 1) & 1][e & 1] * wsc2, tb = wb[e >> 2][(e >> 1) & 1][e & 1] * wsc2;
        asm("" : "+v"(ta), "+v"(tb));
        wa[e >> 2][(e >> 1) & 1][e & 1] = ta; wb[e >> 2][(e >> 1) & 1][e & 1] = tb;
      }
    }
    await<0>(SY(C_GOA), 1);
    if (act) {
      u4 b[4];
#pragma unroll
      for (int c = 0; c < 4; ++c) {
        const int cc = (c >> 1) | ((c & 1) << 1);
#pragma unroll
        for (int u = 0; u < 4; ++u) b[cc][u] = ld32<SC1>(rt, vo, (16 * u + cc) * TP * 4);
      }
      u4 b2[4];
      dft64(ar, ai, b, [&](int r, float (&x)[8]) {
        float y[8];
#pragma unroll
        for (int u = 0; u < 4; ++u) {
          const float a = wa[u][r >> 1][r & 1], c = wb[u][r >> 1][r & 1];
          y[2 * u] = fmaf(x[2 * u], a, -(x[2 * u + 1] * c)); y[2 * u + 1] = fmaf(-x[2 * u], c, -(x[2 * u + 1] * a));
        }
        OPAQUE8(y);
#pragma unroll
        for (int u = 0; u < 4; ++u) b2[r][u] = pk(y[2 * u], y[2 * u + 1]);
      });
      dft64(ar, ai, b2, [&](int r, float (&x)[8]) {
#pragma unroll
        for (int u = 0; u < 4; ++u) st32<SC1>(pk(x[2 * u], x[2 * u + 1]) | 1u, rt, vo, (16 * u + r) * TP * 4);
      });
    } else {
      __builtin_amdgcn_s_sleep(40);
    }
    __builtin_amdgcn_s_setprio(0);
    arrive(SY(C_A2D));
  } else {
    if (w == 15) {
      if (l < 31) *(unsigned*)(smem + L_SYNC + 4 * l) = 0u;
      unsigned zero = 0;
      PIN(zero);
      if (l == 0 && blockIdx.x < 72)
        __hip_atomic_fetch_or((GLB unsigned*)(ws + WS_FLAG + 128 * blockIdx.x), zero, __ATOMIC_RELAXED, __HIP_MEMORY_SCOPE_AGENT);
    }
    if (w == 13) {
      const float kf = (float)n32, a32 = kf * (1.f / 32.f), a640 = kf * (1.f / 640.f);
      u4 c, s;
#pragma unroll
      for (int jj = 0; jj < 4; ++jj) {
        const float a = a32 * (float)(8 * h + 2 * jj), b = a + a32;
        c[jj] = pk(fcos(a), fcos(b)); s[jj] = pk(fsin(a), fsin(b));
      }
      FRAG(F_C0) = c; FRAG(F_S0) = s;
#pragma unroll
      for (int rg = 0; rg < 3; ++rg) {
        f4 tc, tn;
#pragma unroll
        for (int jj = 0; jj < 4; ++jj) { const float a = a640 * (float)(jj + 8 * rg + 4 * h); tc[jj] = fcos(a); tn[jj] = fsin(a); }
        FRAGF(F_TC + rg) = tc; FRAGF(F_TS + rg) = tn;
      }
    }
    if (w == 14) {
      u4 m3[2];
      const float scale = 2.f / (2048.f * 640.f);
      const float sf = (float)(64 * (8 * q + (i >> 1)) + s2) * (1.f / 2048.f);
      const float ph = -0.25f * (float)(i & 1);
#pragma unroll
      for (int j = 0; j < 8; ++j) {
        const int jr = (j + 2 * q) & 7;
        const float a = fmaf(sf, (float)(16 * (jr >> 2) + (jr & 3) - 16 + 4 * g), ph);
        m3[j >> 2][j & 3] = pk(fcos(a) * scale, fsin(a) * scale);
      }
      FRAG(F_M3) = m3[0]; FRAG(F_M3 + 1) = m3[1];
    }
    LBAR();
    await<0>(SY(C_XI), 5);
    const int gk = w - 13, ck = 80 * xcd + 5 * (idx >> 1) + (gk == 2 ? 2 : gk + 3 * (idx & 1));
    const rsrc rk = mkrs(ssk + ck * 2048, 4096);
    const unsigned dk = __builtin_amdgcn_raw_buffer_load_b16(mkrs(ssd, 1280), 2 * ck, 0, 0);
    u4 kc[4]; u2 kp[4];
#pragma unroll
    for (int j = 0; j < 4; ++j) { kc[j] = ld128<NT>(rk, 16 * l + 1024 * j, 0); kp[j] = ld64<NT>(rk, j ? 16 * l + 1024 * j - 8 : 16 * l - 8, 0); }
    SCHED();
    if (l == 0) kc[0][0] = pk(blo(kc[0][0]) + blo(dk), bhi(kc[0][0]));
    char* base = smem + L_GRP + gk * GRP;
    const u4 z4 = {0u, 0u, 0u, 0u};
    if (l < 40) *(u4*)(base + (l < 8 ? (l >> 1) * KCS + 16 * (l & 1) : G_U + 4096 + 16 * (l - 8))) = z4;
#pragma unroll
    for (int j = 0; j < 4; ++j) {
      const unsigned o0 = __builtin_amdgcn_alignbit(kp[j][1], kp[j][0], 16), o1 = __builtin_amdgcn_alignbit(kc[j][0], kp[j][1], 16),
                     o2 = __builtin_amdgcn_alignbit(kc[j][1], kc[j][0], 16), o3 = __builtin_amdgcn_alignbit(kc[j][2], kc[j][1], 16),
                     o4 = __builtin_amdgcn_alignbit(kc[j][3], kc[j][2], 16);
      char* kp0 = base + 32 + 16 * l + 1024 * j;
      const u4 c1 = {o1, o2, o3, o4}, c2 = {kp[j][1], kc[j][0], kc[j][1], kc[j][2]}, c3 = {o0, o1, o2, o3};
      *(u4*)(kp0) = kc[j]; *(u4*)(kp0 + KCS) = c1; *(u4*)(kp0 + 2 * KCS) = c2; *(u4*)(kp0 + 3 * KCS) = c3;
    }
    arrive(SY(C_K + gk));
    conv = true;
  }

  if (conv) {
    LANES();
    const int wl = w & 3, t = tid & 255;
    char* base = smem + L_GRP + gi * GRP;
    char* ul = base + G_U;
    const rsrc ru = mkrs(ws + WS_KV + (96 * xcd + 3 * idx + (gi == 2 ? 2 - 2 * (idx & 1) : gi + (idx & 1))) * CP, 4096);
    int ao = (3 - (i >> 2)) * KCS + 16 * g + 8 * (i & 3) + 8 + 128 * wl;
    int bo = G_U + 4064 - 3584 - 32 * i + 16 * g + 128 * wl;
    int wo = wl * 1024 + 16 * (4 * i + ((g + (i >> 1)) & 3));
    const int c = (t >> 1) & 15, r0 = (wl < 2 ? G_P : -16384) + 4096 * (t >> 5) + 64 * c;
    int p0 = r0 + 16 * ((2 * (t & 1) + (c >> 1)) & 3), p1 = r0 + 16 * ((2 * (t & 1) + 1 + (c >> 1)) & 3);
    const int mask = gi < 2 ? 0xff : ((idx & 1) ? 0x3b : 0xc4);
    int yo = ((mask >> (t >> 5)) & 1) ? 4080 - 16 * t : 4096;
    PIN(ao); PIN(bo); PIN(wo); PIN(p0); PIN(p1); PIN(yo);
    if (w < 13) {
      drain();
      arrive(w < 4 ? SY(C_A1D) : SY(C_B1D));
    }
    u4 a0[8];
    auto preload = [&]() {
      await<1>(SY(C_K + gi), 1);
#pragma unroll
      for (int e = 0; e < 8; ++e) { a0[e] = cat(trread(base + ao + 512 * e), trread(base + ao + 512 * e + 8)); PIN(a0[e]); }
    };
    if (w != 12) preload();
    if (w == 12) {
      const unsigned tag = *(const unsigned*)(smem + SY(C_TAG));
      await<0>(SY(C_B1D), 5);
      if (l == 0) st32<SC1>(tag, rf, FL_B1 + 128 * cgp + 4 * rb, 0);
      preload();
      pace(tstart, XB, 30);
      gather<false>(rf, FL_B1 + 128 * xcd + 4 * n32, tag);
      arrive(SY(C_GOB));
    }
    __builtin_amdgcn_s_setprio(3);
    await<1>(SY(C_GOB), 1);
    const u4 uv = ld128<SC1>(ru, 16 * t, 0);
    *(u4*)(ul + 16 * t) = uv;
    const int cg = SY(C_G + gi);
    arrive(cg);
    await<0>(cg, 4);
    if (gi == 2) __builtin_amdgcn_s_setprio(2);
    if (gi == 0) __builtin_amdgcn_s_setprio(1);
    if (gi == 1) __builtin_amdgcn_s_setprio(0);
    if (gi < 2) conv_part<0xff>(base, ao, bo, wo, cg, a0);
    else if (idx & 1) conv_part<0x3b>(base, ao, bo, wo, cg, a0);
    else conv_part<0xc4>(base, ao, bo, wo, cg, a0);
    arrive(cg);
    await<0>(cg, 12);
    f4 pa[4], pb[4];
#pragma unroll
    for (int ww = 0; ww < 4; ++ww) { pa[ww] = *(const f4*)(base + p0 + ww * 1024); pb[ww] = *(const f4*)(base + p1 + ww * 1024); }
    float sm[8], sn[8];
#pragma unroll
    for (int e = 0; e < 4; ++e) { sm[e] = pa[0][e] + pa[1][e]; sm[4 + e] = pb[0][e] + pb[1][e]; sn[e] = pa[2][e] + pa[3][e]; sn[4 + e] = pb[2][e] + pb[3][e]; }
    OPAQUE8(sm); OPAQUE8(sn);
#pragma unroll
    for (int e = 0; e < 8; ++e) sm[e] += sn[e];
    OPAQUE8(sm);
    const u4 o = {pk(sm[7], sm[6]), pk(sm[5], sm[4]), pk(sm[3], sm[2]), pk(sm[1], sm[0])};
    st128<SC1>(o | 1u, ru, yo);
    __builtin_amdgcn_s_setprio(0);
    arrive(SY(C_B2D));
  }

  if (w < 8) {
    LANES();
    u4 C0 = FRAG(F_C0), S0 = FRAG(F_S0), QC = FRAG(F_QC), QS = FRAG(F_QS), QM = FRAG(F_QM);
    f4 tc[3], tn[3];
#pragma unroll
    for (int j = 0; j < 3; ++j) { tc[j] = FRAGF(F_TC + j); tn[j] = FRAGF(F_TS + j); PIN(tc[j]); PIN(tn[j]); }
    u4 N0 = S0 ^ SGN2;
    PIN(C0); PIN(S0); PIN(N0); PIN(QC); PIN(QS); PIN(QM);
    int zo = L_U + 2 * w * ZP + 40 * (8 * h + ((l >> 2) & 3)) + 8 * min(4 * ((l >> 4) & 1) + (l & 3), 4);
    PIN(zo);
    if (w < 6) {
      u4 m3[2] = {FRAG(F_M3), FRAG(F_M3 + 1)};
      const rsrc rt = mkrs(ws + WS_T + s2 * TP * 4, TP * 4);
      int vo = 4 * (4 * g * SLOT + (w < 5 ? 64 * w + 4 * i : 320));
      int wo = L_U + 4 * g * ZP + 128 * w + 8 * i;
      PIN(m3[0]); PIN(m3[1]); PIN(vo); PIN(wo);
      await<2>(SY(C_A2D), 3);
      __builtin_amdgcn_s_sleep(D_X2);
      pace(tstart, X2A, 0);
      __builtin_amdgcn_s_setprio(3);
      const f4 z = {0.f, 0.f, 0.f, 0.f};
      bool bad = true;
      if (w < 5) {
        u4 tv[8];
        SPIN_LOOP {
          CFENCE();
          if (bad) {
            unsigned m = 1u;
#pragma unroll
            for (int j = 0; j < 8; ++j) { const int jr = (j + 2 * q) & 7; tv[j] = ld128<SC1>(rt, vo, 4 * ((jr & 3) * SLOT + (jr < 4 ? 31 : 16 * SLOT))); }
#pragma unroll
            for (int j = 0; j < 8; ++j) m = and4(m, tv[j]);
            bad = m == 0u;
          }
          if (!__any(bad)) break;
          __builtin_amdgcn_s_sleep(D_RETRY);
        }
        f4 acc[4];
#pragma unroll
        for (int t = 0; t < 4; ++t) {
          const u4 b0 = {tv[0][t], tv[1][t], tv[2][t], tv[3][t]}, b1 = {tv[4][t], tv[5][t], tv[6][t], tv[7][t]};
          acc[t] = mfma16(m3[1], b1, mfma16(m3[0], b0, z));
        }
#pragma unroll
        for (int r = 0; r < 4; ++r) {
          const u2 o = {pk(acc[0][r], acc[1][r]), pk(acc[2][r], acc[3][r])};
          *(u2*)(smem + wo + r * ZP) = o;
        }
      } else {
        u4 nv[2];
        SPIN_LOOP {
          CFENCE();
#pragma unroll
          for (int j = 0; j < 8; ++j) { const int jr = (j + 2 * q) & 7; nv[j >> 2][j & 3] = ld32<SC1>(rt, vo, 4 * ((jr & 3) * SLOT + (jr < 4 ? 31 : 16 * SLOT))); }
          if (__all(and4(and4(1u, nv[0]), nv[1]) != 0u)) break;
          __builtin_amdgcn_s_sleep(D_RETRY);
        }
        const f4 acc = mfma16(m3[1], nv[1], mfma16(m3[0], nv[0], z));
        *(float*)(smem + wo) = acc[0]; *(float*)(smem + wo + 2 * ZP) = acc[2];
      }
      arrive(SY(C_A3));
    }
    await<0>(SY(C_A3), 6);
    {
      const char* zp = smem + zo;
      const u4 are = cat(trread(zp), trread(zp + 160)), aim = cat(trread(zp + ZP), trread(zp + ZP + 160));
      const float nyq = *(const float*)(smem + L_U + 2 * w * ZP + 640);
      f16v vr = {}, vi = {};
      vr = mfma32(are, C0, vr); vi = mfma32(are, S0, vi);
      vr = mfma32(aim, N0, vr); vi = mfma32(aim, C0, vi);
      f16v y;
#pragma unroll
      for (int r = 0; r < 16; ++r) y[r] = (r & 1) ? -nyq : nyq;
      u4 br, bi, bm;
      twiddle<true>(vr, vi, tc, tn, br, bi, bm);
      y = mfma32(br, QC, y); y = mfma32(bi, QS, y); y = mfma32(bm, QM, y);
      const rsrc ro = mkrs(out + (64 * (8 * q + w) + s2) * 1280, 1280);
      char* zr = smem + L_U + 2 * w * ZP;
      if (n32 < 20) {
#pragma unroll
        for (int t = 0; t < 4; ++t) {
          const u2 o = {pk(y[4 * t], y[4 * t + 1]), pk(y[4 * t + 2], y[4 * t + 3])};
          *(u2*)(zr + 72 * n32 + 8 * h + 16 * t) = o;
        }
      }
      const char* zs = zr + 72 * (l >> 2) + 16 * (l & 3);
      const u4 o0 = cat(*(const u2*)zs, *(const u2*)(zs + 8)), o1 = cat(*(const u2*)(zs + 72 * 16), *(const u2*)(zs + 72 * 16 + 8));
      st128<SC1>(o0, ro, 16 * l);
      st128<SC1>(o1, ro, 16 * l + 1024);
    }
    if (w == 4 && blockIdx.x == 0 && l == 0) st32<SC1>(*(const unsigned*)(smem + SY(C_TAG)), rf, FL_EPOCH, 0);
  } else if (w < 13) {
    LANES();
    const int t = tid - 512;
    constexpr int L_Y = L_GRP;
    u4 yv[2];
    const rsrc ry = mkrs(ws + WS_KV + 96 * cgp * CP + 3968 - 128 * rb, 0x7fffffff);
    int yi = ((t >> 3) + ((t >> 3) + 2 + ((0x3b >> (rb >> 2)) & 1)) / 5) * CP + 16 * (t & 7);
    PIN(yi);
    await<2>(SY(C_B2D), 12);
    __builtin_amdgcn_s_sleep(D_X2);
    pace(tstart, X2B, 0);
#pragma unroll
    for (int j = 0; j < 2; ++j) yv[j] = ld128<SC1>(ry, yi, j * 48 * CP);
    SCHED();
    const int gq = t >> 4, blk = gq / 10, c8 = gq % 10;
    await<0>(SY(C_Q), 1);
    f2 qf[2][4];
#pragma unroll
    for (int j = 0; j < 2; ++j) {
      const u4 qv = *(const u4*)(smem + L_IMG + 16 * t + 5120 * j);
#pragma unroll
      for (int e = 0; e < 4; ++e) { qf[j][e] = unp(qv[e]); PIN(qf[j][e]); }
    }
    int wo = L_Y + (t >> 3) * 144 + 16 * (t & 7);
    int po = L_Y + (8 * c8 + (i >> 2)) * 144 + 32 * blk + 8 * (i & 3), oo = (31 - 16 * blk - i) * 2560 + 16 * c8;
    PIN(wo); PIN(po); PIN(oo);
    SCHED();
    SPIN_LOOP {
      const bool bad = and4(and4(1u, yv[0]), yv[1]) == 0u;
      if (!__any(bad)) break;
      __builtin_amdgcn_s_sleep(D_RETRY);
      CFENCE();
      if (bad) {
#pragma unroll
        for (int j = 0; j < 2; ++j) yv[j] = ld128<SC1>(ry, yi, j * 48 * CP);
      }
    }
#pragma unroll
    for (int j = 0; j < 2; ++j) *(u4*)(smem + wo + 40 * 144 * j) = yv[j];
    arrive(SY(C_B1));
    await<0>(SY(C_B1), 10);
    const rsrc ro = mkrs(out + 64 * rb * 1280 + 640 + 80 * cgp, 0x7fffffff);
#pragma unroll
    for (int j = 0; j < 2; ++j) {
      const char* p = smem + po + 64 * j;
      const u4 y = cat(trread(p), trread(p + 4 * 144));
      u4 o;
      float v[8];
#pragma unroll
      for (int e = 0; e < 4; ++e) { v[2 * e] = blo(y[e]) * qf[j][e][0]; v[2 * e + 1] = bhi(y[e]) * qf[j][e][1]; }
      OPAQUE8(v);
#pragma unroll
      for (int e = 0; e < 4; ++e) o[e] = pk(v[2 * e], v[2 * e + 1]);
      st128<0>(o, ro, oo + (1 - j) * 32 * 2560);
    }
  }
}

extern "C" __device__ __attribute__((used, noinline)) void gfh3_code_end() {}

extern "C" void kernel_launch(void* const* d_in, const int* in_sizes, int n_in, void* d_out, int out_size, void* d_ws,
                              size_t ws_size, hipStream_t stream) {
  gfh3<<<256, 1024, 0, stream>>>((const uint16_t*)d_in[0], (const uint16_t*)d_in[1], (const uint16_t*)d_in[2], (const uint16_t*)d_in[3],
                                 (const uint16_t*)d_in[4], (const uint16_t*)d_in[5], (const uint16_t*)d_in[6], (const uint16_t*)d_in[7],
                                 (const uint16_t*)d_in[8], (uint16_t*)d_out, (char*)d_ws);
}
```

```cpp
#include <hip/hip_runtime.h>
#include <stdint.h>
#include <utility>

typedef __attribute__((ext_vector_type(2))) __bf16 bf2;
typedef __attribute__((ext_vector_type(8))) __bf16 bf8;
typedef __attribute__((ext_vector_type(2))) float f2;
typedef __attribute__((ext_vector_type(4))) float f4;
typedef __attribute__((ext_vector_type(16))) float f16v;
typedef __attribute__((ext_vector_type(2))) unsigned u2;
typedef __attribute__((ext_vector_type(4))) unsigned u4;
typedef __attribute__((ext_vector_type(4))) short s4;
typedef __amdgpu_buffer_rsrc_t rsrc;
#define DI __device__ __forceinline__
#define LDS __attribute__((address_space(3)))
#define GLB __attribute__((address_space(1)))
#define PIN(x) asm volatile("" : "+v"(x))
#define SCHED() __builtin_amdgcn_sched_barrier(0)
#define BAR() asm volatile("s_barrier" ::: "memory")
#define LBAR() asm volatile("s_waitcnt lgkmcnt(0)\n\ts_barrier" ::: "memory")
#define CFENCE() asm volatile("" ::: "memory")
#define SPIN_LOOP _Pragma("unroll 1") for (unsigned spin = 0; spin < SPIN_MAX; ++spin)

#define XA 490
#define XB 418
#define X2A 768
#define X2B 825
constexpr int D_X2 = 6, D_RETRY = 4;
constexpr int NT = 2;
constexpr int SC1 = 16;
constexpr int NCOL = 32 * 321;
constexpr int SLOT = 352;
constexpr int TP = 32 * SLOT + 32;
constexpr int CP = 4096 + 128;
constexpr unsigned SGN2 = 0x80008000u;
constexpr unsigned SPIN_MAX = 1u << 18;

constexpr int WS_T = 0, WS_KV = 64 * TP * 4, WS_FLAG = WS_KV + 768 * CP, FL_A1 = 0, FL_B1 = 8 * 1024, FL_EPOCH = 9 * 1024;

enum { F_C0, F_S0, F_PC, F_PS, F_MR, F_QC, F_QS, F_QM, F_TC, F_TS = F_TC + 3, F_AR = F_TS + 3, F_M3 = F_AR + 4, F_ALL = F_M3 + 2 };

DI unsigned pk(float a, float b) { f2 v = {a, b}; return __builtin_bit_cast(unsigned, __builtin_convertvector(v, bf2)); }
DI float blo(unsigned v) { return __uint_as_float(v << 16); }
DI float bhi(unsigned v) { return __uint_as_float(v & 0xffff0000u); }
DI f2 unp(unsigned v) { f2 r = {blo(v), bhi(v)}; return r; }
DI float fcos(float r) { return __builtin_amdgcn_cosf(r); }
DI float fsin(float r) { return __builtin_amdgcn_sinf(r); }
DI f4 mfma16(u4 a, u4 b, f4 c) { return __builtin_amdgcn_mfma_f32_16x16x32_bf16(__builtin_bit_cast(bf8, a), __builtin_bit_cast(bf8, b), c, 0, 0, 0); }
DI f16v mfma32(u4 a, u4 b, f16v c) { return __builtin_amdgcn_mfma_f32_32x32x16_bf16(__builtin_bit_cast(bf8, a), __builtin_bit_cast(bf8, b), c, 0, 0, 0); }
DI u2 trread(const char* p) { return __builtin_bit_cast(u2, __builtin_amdgcn_ds_read_tr16_b64_v4i16((LDS s4*)p)); }
DI u4 cat(u2 a, u2 b) { u4 r = {a[0], a[1], b[0], b[1]}; return r; }
DI rsrc mkrs(const void* p, unsigned bytes) { return __builtin_amdgcn_make_buffer_rsrc((void*)p, 0, bytes, 0x00020000); }
template <int AUX = 0> DI u4 ld128(rsrc r, int v, int s) { return __builtin_bit_cast(u4, __builtin_amdgcn_raw_buffer_load_b128(r, v, s, AUX)); }
template <int AUX = 0> DI unsigned ld32(rsrc r, int v, int s) { return __builtin_amdgcn_raw_buffer_load_b32(r, v, s, AUX); }
template <int AUX> DI void st128(u4 x, rsrc r, int v) { __builtin_amdgcn_raw_buffer_store_b128(x, r, v, 0, AUX); }
template <int AUX> DI void st32(unsigned x, rsrc r, int v, int s) { __builtin_amdgcn_raw_buffer_store_b32(x, r, v, s, AUX); }
DI unsigned and4(unsigned m, u4 a) { return m & a[0] & a[1] & a[2] & a[3]; }
DI void drain() { asm volatile("s_waitcnt vmcnt(0)" ::: "memory"); }
template <int H> DI f2 half(const f4& a) { return __builtin_shufflevector(a, a, 2 * H, 2 * H + 1); }
template <int H> DI f2 half(const f16v& a) { return __builtin_shufflevector(a, a, 2 * H, 2 * H + 1); }

DI void arrive(int off) {
  SCHED();
  asm volatile("s_mov_b64 exec, 1\n\tds_add_u32 %0, %1\n\ts_mov_b64 exec, -1" :: "v"(off), "v"(1u) : "memory");
  SCHED();
}
template <int SLEEP> DI void await(int off, int n) {
  unsigned v = 0, s;
  asm volatile("1:\n\tds_read_b32 %0, %2\n\ts_waitcnt lgkmcnt(0)\n\tv_readfirstlane_b32 %1, %0\n\ts_cmp_ge_u32 %1, %3\n\t"
               "s_cbranch_scc1 2f\n\ts_sleep %4\n\ts_branch 1b\n2:" : "+&v"(v), "=&s"(s) : "v"(off), "i"(n), "i"(SLEEP) : "memory", "scc");
}

constexpr int KCS = 4160;
constexpr int G_P = 4 * KCS, G_U = G_P + 16384;
constexpr int GRP = G_U + 4608;
constexpr int ZP = 768;
constexpr int L_SYNC = 0, L_TAB = 128, L_U = L_TAB + F_ALL * 1024, L_IMG = L_U + 12 * 1280, L_GRP = L_IMG + 10240, L_END = L_GRP + 3 * GRP;
enum { C_GOA, C_GOB, C_A1, C_A1D, C_B1, C_B1S, C_B1D, C_K, C_G = C_K + 3, C_A2D = C_G + 3, C_A3, C_B2D, C_PUBA, C_XI, C_Q, C_WL, C_TI, C_TAG = 31 };
#define SY(c) (L_SYNC + 4 * (c))

#define OPAQUE8(a) asm("" : "+v"((a)[0]), "+v"((a)[1]), "+v"((a)[2]), "+v"((a)[3]), "+v"((a)[4]), "+v"((a)[5]), "+v"((a)[6]), "+v"((a)[7]))
template <typename F> DI void dft64(const u4 (&ar)[4], const u4 (&ai)[4], const u4 (&b)[4], F f) {
  f4 er[4], ei[4];
  const f4 z = {0.f, 0.f, 0.f, 0.f};
#pragma unroll
  for (int c = 0; c < 4; ++c) { const int cc = (c >> 1) | ((c & 1) << 1); er[cc] = mfma16(ar[cc], b[cc], z); ei[cc] = mfma16(ai[cc], b[cc], z); }
#pragma unroll
  for (int rr = 0; rr < 4; ++rr) {
    const int r = (rr >> 1) | ((rr & 1) << 1);
    float t[8] = {er[0][r] + er[2][r], ei[0][r] + ei[2][r], er[0][r] - er[2][r], ei[0][r] - ei[2][r],
                  er[1][r] + er[3][r], ei[1][r] + ei[3][r], er[1][r] - er[3][r], ei[1][r] - ei[3][r]};
    OPAQUE8(t);
    float x[8] = {t[0] + t[4], t[1] + t[5], t[2] + t[7], t[3] - t[6], t[0] - t[4], t[1] - t[5], t[2] - t[7], t[3] + t[6]};
    OPAQUE8(x);
    f(r, x);
  }
}

template <typename F, int... I> DI void static_for_(F f, std::integer_sequence<int, I...>) { (f(std::integral_constant<int, I>{}), ...); }
template <int N, typename F> DI void static_for(F f) { static_for_(f, std::make_integer_sequence<int, N>{}); }
template <bool INV> DI void twiddle(const f16v& vr, const f16v& vi, const f4 (&tc)[3], const f4 (&tn)[3], u4& br, u4& bi, u4& bm) {
  unsigned pr[6], pi[6];
  f2 re[6], im[6];
  static_for<6>([&](auto jc) {
    constexpr int J = decltype(jc)::value;
    re[J] = half<J>(vi) * half<J & 1>(tn[J >> 1]); im[J] = half<J>(vr) * half<J & 1>(tn[J >> 1]);
  });
  SCHED();
  static_for<6>([&](auto jc) {
    constexpr int J = decltype(jc)::value;
    re[J] = __builtin_elementwise_fma(half<J>(vr), half<J & 1>(tc[J >> 1]), INV ? -re[J] : re[J]);
    im[J] = __builtin_elementwise_fma(half<J>(vi), half<J & 1>(tc[J >> 1]), INV ? im[J] : -im[J]);
  });
  SCHED();
#pragma unroll
  for (int j = 0; j < 6; ++j) { pr[j] = pk(re[j][0], re[j][1]); pi[j] = pk(im[j][0], im[j][1]); }
  br = (u4){pr[0], pr[1], pr[2], pr[3]}; bi = (u4){pi[0], pi[1], pi[2], pi[3]}; bm = (u4){pr[4], pr[5], pi[4], pi[5]};
}

struct ConvList { int n, a[36], b[36]; };
constexpr ConvList conv_list(int mask) {
  ConvList c{};
  for (int s = 0; s < 8; ++s) {
    for (int e = 0; e <= s; ++e) if (s + e < 8 && ((mask >> (s + e)) & 1)) { c.a[c.n] = s; c.b[c.n] = e; ++c.n; }
    for (int e = 0; e < s; ++e) if (s + e < 8 && ((mask >> (s + e)) & 1)) { c.a[c.n] = e; c.b[c.n] = s; ++c.n; }
  }
  return c;
}
constexpr int conv_rd(int n) {
  constexpr int RB[5] = {0, 4, 10, 14, 17};
  for (int f = 0; f < 5; ++f) if (n == RB[f]) return 3 * (3 + f);
  constexpr int RF[8] = {21, 24, 27, 32, 36, 39, 42, 45};
  for (int f = 0; f < 8; ++f) if (n >= RF[f] && n < RF[f] + 3) return 3 * (8 + f) + n - RF[f];
  return -1;
}
constexpr int conv_wr(int n) {
  constexpr int WF[7] = {49, 51, 53, 57, 63, 68, 71};
  for (int j = 0; j < 7; ++j) if (n == WF[j]) return j;
  return -1;
}
template <int MASK> DI void conv_part(char* base, int ao, int bo, int wo, int cg, const u4 (&a0)[8]) {
  constexpr int NB = (MASK & 0x80) ? 8 : 6;
  constexpr ConvList L = conv_list(MASK);
  constexpr bool FULL = MASK == 0xff;
  constexpr int PRO = FULL ? 3 : 4;
  f4 acc[8];
#pragma unroll
  for (int j = 0; j < 8; ++j) acc[j] = (f4){0.f, 0.f, 0.f, 0.f};
  u4 b[2 * NB]; u2 al[NB], ah[NB];
  auto rd = [&](int f, int part) {
    const int rr = f / NB, e = f % NB;
    if (part == 0) b[f] = *(const u4*)(base + bo + 512 * (7 - e) + 64 * rr);
    if (part == 1) al[e] = trread(base + ao + 512 * e + 64);
    if (part == 2) ah[e] = trread(base + ao + 512 * e + 64 + 8);
  };
  auto wr = [&](int j) { if ((MASK >> j) & 1) *(f4*)(base + wo + (j < 4 ? G_P + 4096 * j : 4096 * (j - 4))) = acc[j]; };
#pragma unroll
  for (int f = 0; f < PRO; ++f) { rd(f, 0); SCHED(); }
  constexpr int R1 = 9;
  constexpr int LASTRD = FULL ? 47 : R1 + (3 * NB - 1) / 2;
  unsigned seen = 0;
  static_for<2 * L.n>([&](auto nc) {
    constexpr int n = decltype(nc)::value, o = n < L.n ? 0 : NB, fa = L.a[n % L.n], fb = L.b[n % L.n];
    acc[fa + fb] = mfma16(n < L.n ? a0[fa] : cat(al[fa], ah[fa]), b[o + fb], acc[fa + fb]);
    SCHED();
    if constexpr (FULL) {
      constexpr int f = conv_rd(n), j = conv_wr(n);
      if constexpr (f >= 0) rd(f / 3, f % 3);
      if constexpr (n == 58) seen = *(volatile LDS unsigned*)(size_t)cg;
      if constexpr (j == 4) { if (__builtin_amdgcn_readfirstlane(seen) < 8) await<0>(cg, 8); }
      if constexpr (j >= 0) wr(j);
    } else {
      if constexpr (n < NB - PRO) rd(PRO + n, 0);
      constexpr int k = 2 * (n - R1);
      if constexpr (k >= 0 && k < 3 * NB) { rd(NB + k / 3, k % 3); rd(NB + (k + 1) / 3, (k + 1) % 3); }
    }
    if constexpr (n == LASTRD + 1) arrive(cg);
    SCHED();
  });
  if (FULL) wr(7);
  else {
    await<0>(cg, 8);
#pragma unroll
    for (int j = 0; j < 8; ++j) wr(j);
  }
}

template <bool STREAM> DI void glds16(const void* src, int dst) {
  unsigned keep;
  if (STREAM) asm volatile("s_mov_b32 %0, m0\n\ts_mov_b32 m0, %2\n\ts_nop 0\n\tglobal_load_lds_dwordx4 %1, off nt\n\ts_mov_b32 m0, %0"
                           : "=&s"(keep) : "v"(src), "s"(dst) : "memory");
  else asm volatile("s_mov_b32 %0, m0\n\ts_mov_b32 m0, %2\n\ts_nop 0\n\tglobal_load_lds_dwordx4 %1, off\n\ts_mov_b32 m0, %0"
                    : "=&s"(keep) : "v"(src), "s"(dst) : "memory");
}
DI unsigned now() { return (unsigned)__builtin_amdgcn_s_memrealtime(); }
DI void pace(unsigned t0, int x, int p) {
  const unsigned t1 = now();
  while ((int)(now() - t0) < x || (int)(now() - t1) < p) __builtin_amdgcn_s_sleep(1);
}
template <bool WIDE> DI void gather(rsrc rf, int off, unsigned tag) {
  SPIN_LOOP {
    CFENCE();
    bool ok;
    if (WIDE) { const u4 f = ld128<SC1>(rf, off, 0); ok = f[0] == tag && f[1] == tag && f[2] == tag && f[3] == tag; }
    else ok = ld32<SC1>(rf, off, 0) == tag;
    if (__all(ok)) break;
    __builtin_amdgcn_s_sleep(2);
  }
}

DI int fresh_lane() {
  unsigned z = 0;
  PIN(z);
  return __builtin_amdgcn_mbcnt_hi(~0u, __builtin_amdgcn_mbcnt_lo(~0u, z));
}
#define LANES() const int l = fresh_lane(), tid = 64 * w + l, i = l & 15, g = l >> 4, n32 = l & 31, h = l >> 5; char* tab = smem + L_TAB + 16 * l; \
  (void)tid; (void)i; (void)g; (void)n32; (void)h; (void)tab

extern "C" __global__ __launch_bounds__(1024, 4) void gfh3(const uint16_t* __restrict__ x, const uint16_t* __restrict__ wr, const uint16_t* __restrict__ wi,
                                                        const uint16_t* __restrict__ qin, const uint16_t* __restrict__ kin, const uint16_t* __restrict__ vin,
                                                        const uint16_t* __restrict__ shk, const uint16_t* __restrict__ ssk, const uint16_t* __restrict__ ssd,
                                                        uint16_t* __restrict__ out, char* ws) {
  __shared__ __attribute__((aligned(16))) char smem[L_END];
  const int l = threadIdx.x & 63;
  const int wv0 = (__builtin_amdgcn_readfirstlane(threadIdx.x >> 6) + 8) & 15;
  const int w = wv0 >= 12 ? wv0 ^ 1 : wv0;
  const int tid = 64 * w + l;
  const int xcd = blockIdx.x & 7, idx = blockIdx.x >> 3;
  const int q = idx & 3, s2 = xcd * 8 + (idx >> 2);
  const int rb = 8 * xcd + (idx >> 2), cgp = idx & 3;
  const int i = l & 15, g = l >> 4, n32 = l & 31, h = l >> 5;
  char* tab = smem + L_TAB + 16 * l;
#define FRAG(f) (*(u4*)(tab + 1024 * (f)))
#define FRAGF(f) (*(f4*)(tab + 1024 * (f)))
  const unsigned tstart = now();
  unsigned sink = 0;
  if (w == 8) {
    unsigned long long pc; int rel, len;
    asm volatile("s_getpc_b64 %0\n\ts_mov_b32 %1, gfh3@rel32@lo+4\n\ts_mov_b32 %2, gfh3_code_end-gfh3" : "=s"(pc), "=s"(rel), "=s"(len));
    const char* p = (const char*)(pc + (long long)rel) + min(128 * (8 * l + (idx & 7)), len);
    asm volatile("global_load_dword %0, %1, off" : "+v"(sink) : "v"(p) : "memory");
  }
  asm volatile("" :: "s"(x), "s"(wr), "s"(wi), "s"(qin), "s"(kin), "s"(vin), "s"(shk), "s"(ssk), "s"(ssd), "s"(out), "s"(ws));
  const rsrc rf = mkrs(ws + WS_FLAG, FL_EPOCH + 4);
  const int gi = w < 8 ? 2 : (w - 8) >> 2;
  bool conv = false;

  if (__builtin_expect((unsigned)(w - 8) < 5u, 1)) {
    const int t = tid - 512, c2 = t % 80, tc = t / 80;
    unsigned kr[11], vv[8], epoch;
    u4 wk;
    {
      const rsrc rk = mkrs(kin, 2048 * 1280), rv = mkrs(vin, 2048 * 1280), rs = mkrs(shk, 640 * 8);
      const int vo = (32 * rb + 8 * tc) * 1280 + 320 * cgp + 4 * c2;
      const int vh = (rb | tc) ? vo - 3 * 1280 : 2048 * 1280;
      epoch = ld32<SC1>(rf, w == 8 ? FL_EPOCH : FL_EPOCH + 4, 0);
      SCHED();
#pragma unroll
      for (int j = 0; j < 8; ++j) { kr[3 + j] = ld32<NT>(rk, vo, j * 1280); vv[j] = ld32<NT>(rv, vo, j * 1280); }
      wk = ld128(rs, 1280 * cgp + 16 * c2, 0);
#pragma unroll
      for (int j = 0; j < 3; ++j) kr[j] = ld32<NT>(rk, vh + j * 1280, 0);
      SCHED();
    }
    const rsrc ro = mkrs(ws + WS_KV + 192 * cgp * CP + 4032 - 64 * rb, 191 * CP + 64);
    int so = (t >> 2) * CP + 16 * (t & 3), ri[3];
#pragma unroll
    for (int j = 0; j < 3; ++j) {
      const int r = min((t >> 2) + 80 * j, 191), ch = r - (r + 3) / 6;
      ri[j] = L_IMG + (ch >> 1) * 128 + 16 * ((4 * (ch & 1) + (t & 3) + (ch >> 1)) & 7);
      PIN(ri[j]);
    }
    int wi0 = L_IMG + c2 * 128 + 16 * ((3 - tc + c2) & 7);
    PIN(so); PIN(wi0);
    SCHED();
    asm volatile("" : "+v"(sink), "+v"(epoch));
    if (w == 8) {
      *(unsigned*)(smem + SY(C_TAG)) = epoch * 0x9E3779B1u + 0x7F4A7C15u;
    }
    LBAR();
    __builtin_amdgcn_s_setprio(3);
    {
      const f2 w0 = {blo(wk[0]), blo(wk[2])}, w1 = {bhi(wk[0]), bhi(wk[2])}, w2 = {blo(wk[1]), blo(wk[3])}, w3 = {bhi(wk[1]), bhi(wk[3])};
      f2 kf[11], o[8];
#pragma unroll
      for (int j = 0; j < 11; ++j) kf[j] = unp(kr[j]);
#pragma unroll
      for (int j = 0; j < 8; ++j) o[j] = (kf[j] * w3 + kf[j + 1] * w2 + kf[j + 2] * w1 + kf[j + 3] * w0) * unp(vv[j]);
#pragma unroll
      for (int e = 0; e < 2; ++e) {
        const u4 r = {pk(o[7][e], o[6][e]), pk(o[5][e], o[4][e]), pk(o[3][e], o[2][e]), pk(o[1][e], o[0][e])};
        *(u4*)(smem + (wi0 ^ (64 * e))) = r & ~1u;
      }
    }
    arrive(SY(C_B1));
    await<0>(SY(C_B1), 5);
#pragma unroll
    for (int j = 0; j < 3; ++j) st128<SC1>(*(const u4*)(smem + ri[j]), ro, so + j * 80 * CP);
    arrive(SY(C_B1S));
    conv = true;
  } else if (__builtin_expect(w < 5, 1)) {
    u4 xv[8];
    const rsrc rx = mkrs(x + s2 * 640 + 128 * w, 0x7fffffff);
    const int vx = g * (512 * 1280) + 16 * i;
#pragma unroll
    for (int j = 0; j < 2; ++j) xv[j] = ld128(rx, vx, (j + 2 * q) * 64 * 1280);
    SCHED();
    u4 m1;
    {
      const int wv = i >> 1;
      const float kf = (wv < 4 ? (float)(4 * q + wv + 1) : 0.f) * (1.f / 2048.f);
      const float ph = 0.25f * (float)(i & 1);
#pragma unroll
      for (int jj = 0; jj < 4; ++jj) {
        const float sb = (float)(512 * g + s2 + 128 * ((jj + q) & 3));
        m1[jj] = pk(fcos(fmaf(kf, sb, ph)), fcos(fmaf(kf, sb + 64.f, ph)));
      }
    }
    const int ks1 = w < 4 ? 4 * q + w + 1 : 0;
    const rsrc rt = mkrs(ws + WS_T + s2 * TP * 4, 32 * SLOT * 4);
    const int cs = h ? (16 - ks1) * SLOT + (ks1 ? 0 : TP) : (ks1 + 16) * SLOT;
    int so = 4 * (cs + (h ? (32 - n32) & 31 : n32));
    int sx = 4 * (cs + 320 + n32 + (n32 < 16 ? 0 : TP));
    int upo = L_U + 2 * w * 1280 + 40 * (8 * h + ((l >> 2) & 3)) + 8 * min(4 * ((l >> 4) & 1) + (l & 3), 4);
    int wro = L_U + 4 * g * 1280 + 256 * w + 16 * i;
    PIN(m1); PIN(so); PIN(sx); PIN(upo); PIN(wro);
    BAR();
#pragma unroll
    for (int j = 2; j < 8; ++j) xv[j] = ld128(rx, vx, ((j + 2 * q) & 7) * 64 * 1280);
    arrive(SY(C_XI));
    u4 C0 = FRAG(F_C0), S0 = FRAG(F_S0);
    f4 tc[3], tn[3];
#pragma unroll
    for (int j = 0; j < 3; ++j) { tc[j] = FRAGF(F_TC + j); PIN(tc[j]); }
    const unsigned sg = (n32 & 1) ? SGN2 : 0u;
    u4 N0 = S0 ^ SGN2, C1 = C0 ^ sg, S1 = S0 ^ sg, N1 = N0 ^ sg;
    PIN(C0); PIN(S0); PIN(N0); PIN(C1); PIN(S1); PIN(N1);
    SCHED();
    __builtin_amdgcn_s_setprio(3);
    {
      f4 acc[8];
      u4 b[8];
#pragma unroll
      for (int jj = 0; jj < 4; ++jj) {
#pragma unroll
        for (int t = 0; t < 8; ++t) {
          b[t][jj] = __builtin_amdgcn_perm(xv[2 * jj + 1][t >> 1], xv[2 * jj][t >> 1], (t & 1) ? 0x07060302u : 0x05040100u);
          const f4 z = {0.f, 0.f, 0.f, 0.f};
          if (jj == 3) acc[t] = mfma16(m1, b[t], z);
        }
        SCHED();
      }
      if (g < 3) {
#pragma unroll
        for (int r = 0; r < 4; ++r) {
          u4 o = {pk(acc[0][r], acc[1][r]), pk(acc[2][r], acc[3][r]), pk(acc[4][r], acc[5][r]), pk(acc[6][r], acc[7][r])};
          *(u4*)(smem + wro + r * 1280) = o;
        }
      }
    }
    arrive(SY(C_A1));
    if (w < 4 || q == 0) {
      const u4 PC = FRAG(F_PC), PS = FRAG(F_PS), MR = FRAG(F_MR);
#pragma unroll
      for (int j = 0; j < 3; ++j) tn[j] = FRAGF(F_TS + j);
      await<0>(SY(C_A1), 5);
      const char* up = smem + upo;
      u4 are[2], aim[2];
#pragma unroll
      for (int kk = 0; kk < 2; ++kk) {
        are[kk] = cat(trread(up + 640 * kk), trread(up + 640 * kk + 160));
        aim[kk] = cat(trread(up + 1280 + 640 * kk), trread(up + 1280 + 640 * kk + 160));
      }
      f16v vr = {}, vi = {};
      vr = mfma32(are[0], C0, vr); vi = mfma32(are[0], N0, vi);
      vr = mfma32(aim[0], S0, vr); vi = mfma32(aim[0], C0, vi);
      vr = mfma32(are[1], C1, vr); vi = mfma32(are[1], N1, vi);
      vr = mfma32(aim[1], S1, vr); vi = mfma32(aim[1], C1, vi);
      const u4 PN = PS ^ SGN2, MI = {MR[2] ^ SGN2, MR[3] ^ SGN2, MR[0], MR[1]};
      u4 br, bi, bm;
      twiddle<false>(vr, vi, tc, tn, br, bi, bm);
      f16v xr = {}, xi = {};
      xr = mfma32(PC, br, xr); xi = mfma32(PN, br, xi);
      xr = mfma32(PS, bi, xr); xi = mfma32(PC, bi, xi);
      xr = mfma32(MR, bm, xr); xi = mfma32(MI, bm, xi);
      const unsigned cj = h ? 0x80000000u : 0u;
      unsigned o[10];
#pragma unroll
      for (int r = 0; r < 10; ++r) o[r] = (pk(xr[r], xi[r]) ^ cj) & ~1u;
      const auto sw = __builtin_amdgcn_permlane32_swap(o[9], o[0], false, false);
#pragma unroll
      for (int r = 1; r < 11; ++r) st32<SC1>(l == 32 ? (r < 10 ? o[r - 1] : sw[0] ^ 0x80000000u) : o[r % 10], rt, so + 128 * (r % 10), 0);
      st32<SC1>(h ? o[9] : sw[1] ^ 0x80000000u, rt, sx, 0);
    }
    if (w == 4) {
      LANES();
      drain();
      arrive(SY(C_A1D));
      const unsigned tag = *(const unsigned*)(smem + SY(C_TAG));
      await<1>(SY(C_A1D), 5);
      if (l < 8) st32<SC1>(tag, rf, FL_A1 + 1024 * l + 4 * blockIdx.x, 0);
      arrive(SY(C_PUBA));
      const char* qp[10];
#pragma unroll
      for (int j = 0; j < 10; ++j) {
        const int c = l + 64 * j;
        qp[j] = (const char*)(qin + (32 * rb + c / 20) * 640 + 160 * cgp + 8 * (c % 20));
        PIN(qp[j]);
      }
      await<1>(SY(C_B1D), 5);
      pace(tstart, XA, 25);
      gather<true>(rf, FL_A1 + 1024 * xcd + 16 * l, tag);
      arrive(SY(C_GOA));
      if (idx < 28) await<0>(SY(C_TI), 3);
#pragma unroll
      for (int j = 0; j < 10; ++j) glds16<true>(qp[j], L_IMG + 1024 * j);
      drain();
      arrive(SY(C_Q));
    }
    conv = w < 4;
  } else if (w < 8) {
    const bool act = idx < 28;
    const int slot = 4 * xcd + idx / 7, tile = 3 * (idx % 7) + (w - 5);
    if (w < 7) {
      const int ro = (n32 & 3) + 4 * (n32 >> 3);
      const float a20 = w == 5 ? (float)((n32 & 4) ? 19 - ro : ro) * 0.05f : (float)n32 * -0.05f;
      u4 c, s, m;
#pragma unroll
      for (int jj = 0; jj < 4; ++jj) {
        const float a = a20 * (float)(8 * (jj >> 1) + 4 * h + 2 * (jj & 1)), b = a + a20;
        c[jj] = pk(fcos(a), fcos(b)); s[jj] = pk(fsin(a), fsin(b));
      }
#pragma unroll
      for (int jj = 0; jj < 2; ++jj) {
        const float a = a20 * (float)(16 + 2 * jj), b = a + a20;
        m[jj] = h ? 0u : pk(fcos(a), fcos(b)); m[2 + jj] = h ? 0u : pk(fsin(a), fsin(b));
      }
      char* tp = tab + (w - 5) * 3072;
      *(u4*)(tp + 1024 * F_PC) = c; *(u4*)(tp + 1024 * F_PS) = s; *(u4*)(tp + 1024 * F_MR) = m;
    } else {
      const float a64 = (float)i * (1.f / 64.f);
#pragma unroll
      for (int c = 0; c < 4; ++c) {
        u4 v;
#pragma unroll
        for (int u = 0; u < 4; ++u) { const float a = a64 * (float)(16 * u + c + 4 * g); v[u] = pk(fcos(a), fsin(a)); }
        FRAG(F_AR + c) = v;
      }
    }
    LBAR();
    u4 ar[4], ai[4];
#pragma unroll
    for (int c = 0; c < 4; ++c) {
      ar[c] = FRAG(F_AR + c);
#pragma unroll
      for (int u = 0; u < 4; ++u) ai[c][u] = __builtin_amdgcn_alignbit(ar[c][u], ar[c][u], 16) ^ 0x8000u;
      PIN(ar[c]); PIN(ai[c]);
    }
    const rsrc rt = mkrs(ws + WS_T, 64 * TP * 4);
    int vo = 4 * (4 * g * TP + slot * SLOT + 16 * tile + i);
    const int w0 = (slot - 16) * 321 + 48 * (idx % 7);
    const char* gp[5];
#pragma unroll
    for (int j = 0; j < 5; ++j) {
      const int n = min(3 * j + (w - 5), 13), c = l + 64 * (n % 7);
      int o = 2 * ((w0 & ~7) + (c / 7) * NCOL) + 16 * (c % 7);
      o += (o < 0) ? 128 * NCOL : 0;
      gp[j] = (const char*)(n < 7 ? wr : wi) + min(o, 128 * NCOL - 16);
      PIN(gp[j]);
    }
    PIN(vo);
    if (w < 7) {
      int o = 2 * (1284 * xcd - 16 * 321 - 16 + (2 * idx + h) * NCOL) + 128 * n32;
      o += (o < 0) ? 128 * NCOL : 0;
      await<1>(SY(C_B1S), 5);
      const unsigned t0 = ld32(mkrs(w == 5 ? wr : wi, 128 * NCOL), n32 < 22 ? o : 128 * NCOL, 0);
      asm volatile("" :: "v"(t0));
    }
    await<1>(SY(C_PUBA), 1);
    __builtin_amdgcn_s_setprio(3);
    if (act) {
#pragma unroll
      for (int j = 0; j < 5; ++j)
        if (j < 4 || w < 7) glds16<false>(gp[j], L_U + 1024 * (3 * j + (w - 5)));
    }
    drain();
    arrive(SY(C_WL));
    await<0>(SY(C_WL), 3);
    f2 wa[4][2], wb[4][2];
    const int kd = 16 * tile + i;
    const float wsc2 = (kd == 0 || kd == 320) ? 0.5f : 1.f;
#pragma unroll
    for (int u = 0; u < 4; ++u) {
#pragma unroll
      for (int r = 0; r < 4; ++r) {
        const char* p = smem + L_U + 448 * g + 2 * (i + 16 * (w - 5) + (w0 & 7)) + 1792 * u + 112 * r;
        wa[u][r >> 1][r & 1] = blo(*(const uint16_t*)p); wb[u][r >> 1][r & 1] = blo(*(const uint16_t*)(p + 7168));
      }
      PIN(wa[u][0]); PIN(wb[u][0]); PIN(wa[u][1]); PIN(wb[u][1]);
    }
    if (tile == 0 || tile == 20) {
#pragma unroll
      for (int e = 0; e < 16; ++e) {
        float ta = wa[e >> 2][(e >> 1) & 1][e & 1] * wsc2, tb = wb[e >> 2][(e >> 1) & 1][e & 1] * wsc2;
        asm("" : "+v"(ta), "+v"(tb));
        wa[e >> 2][(e >> 1) & 1][e & 1] = ta; wb[e >> 2][(e >> 1) & 1][e & 1] = tb;
      }
    }
    await<0>(SY(C_GOA), 1);
    if (act) {
      u4 b[4];
#pragma unroll
      for (int c = 0; c < 4; ++c) {
        const int cc = (c >> 1) | ((c & 1) << 1);
#pragma unroll
        for (int u = 0; u < 4; ++u) b[cc][u] = ld32<SC1>(rt, vo, (16 * u + cc) * TP * 4);
      }
      SCHED();
      arrive(SY(C_TI));
      SCHED();
      u4 b2[4];
      dft64(ar, ai, b, [&](int r, float (&x)[8]) {
        float y[8];
#pragma unroll
        for (int u = 0; u < 4; ++u) {
          const float a = wa[u][r >> 1][r & 1], c = wb[u][r >> 1][r & 1];
          y[2 * u] = fmaf(x[2 * u], a, -(x[2 * u + 1] * c)); y[2 * u + 1] = fmaf(-x[2 * u], c, -(x[2 * u + 1] * a));
        }
        OPAQUE8(y);
#pragma unroll
        for (int u = 0; u < 4; ++u) b2[r][u] = pk(y[2 * u], y[2 * u + 1]);
      });
      dft64(ar, ai, b2, [&](int r, float (&x)[8]) {
#pragma unroll
        for (int u = 0; u < 4; ++u) st32<SC1>(pk(x[2 * u], x[2 * u + 1]) | 1u, rt, vo, (16 * u + r) * TP * 4);
      });
    } else {
      __builtin_amdgcn_s_sleep(40);
    }
    __builtin_amdgcn_s_setprio(0);
    arrive(SY(C_A2D));
  } else {
    if (w == 15) {
      if (l < 31) *(unsigned*)(smem + L_SYNC + 4 * l) = 0u;
      unsigned zero = 0;
      PIN(zero);
      if (l == 0 && blockIdx.x < 72)
        __hip_atomic_fetch_or((GLB unsigned*)(ws + WS_FLAG + 128 * blockIdx.x), zero, __ATOMIC_RELAXED, __HIP_MEMORY_SCOPE_AGENT);
    }
    if (w == 13) {
      const float kf = (float)n32, a32 = kf * (1.f / 32.f), a640 = kf * (1.f / 640.f);
      u4 c, s;
#pragma unroll
      for (int jj = 0; jj < 4; ++jj) {
        const float a = a32 * (float)(8 * h + 2 * jj), b = a + a32;
        c[jj] = pk(fcos(a), fcos(b)); s[jj] = pk(fsin(a), fsin(b));
      }
      FRAG(F_C0) = c; FRAG(F_S0) = s;
#pragma unroll
      for (int rg = 0; rg < 3; ++rg) {
        f4 tc, tn;
#pragma unroll
        for (int jj = 0; jj < 4; ++jj) { const float a = a640 * (float)(jj + 8 * rg + 4 * h); tc[jj] = fcos(a); tn[jj] = fsin(a); }
        FRAGF(F_TC + rg) = tc; FRAGF(F_TS + rg) = tn;
      }
    }
    if (w == 14) {
      u4 m3[2];
      const float scale = 2.f / (2048.f * 640.f);
      const float sf = (float)(64 * (8 * q + (i >> 1)) + s2) * (1.f / 2048.f);
      const float ph = -0.25f * (float)(i & 1);
#pragma unroll
      for (int j = 0; j < 8; ++j) {
        const int jr = (j + 2 * q) & 7;
        const float a = fmaf(sf, (float)(16 * (jr >> 2) + (jr & 3) - 16 + 4 * g), ph);
        m3[j >> 2][j & 3] = pk(fcos(a) * scale, fsin(a) * scale);
      }
      FRAG(F_M3) = m3[0]; FRAG(F_M3 + 1) = m3[1];
    }
    LBAR();
    await<0>(SY(C_XI), 5);
    const int gk = w - 13, ck = 80 * xcd + 5 * (idx >> 1) + (gk == 2 ? 2 : gk + 3 * (idx & 1));
    const rsrc rk = mkrs(ssk + ck * 2048, 4096);
    const unsigned dk = __builtin_amdgcn_raw_buffer_load_b16(mkrs(ssd, 1280), 2 * ck, 0, 0);
    u4 kc[4]; u2 kp[4];
#pragma unroll
    for (int j = 0; j < 4; ++j) kc[j] = ld128<NT>(rk, 16 * l + 1024 * j, 0);
    SCHED();
#pragma unroll
    for (int j = 0; j < 4; ++j)
#pragma unroll
      for (int e = 0; e < 2; ++e) {
        const unsigned up = __builtin_amdgcn_update_dpp(0u, kc[j][2 + e], 0x138, 0xf, 0xf, false);
        kp[j][e] = l ? up : (j ? __builtin_amdgcn_readlane(kc[j - 1][2 + e], 63) : 0u);
      }
    if (l == 0) kc[0][0] = pk(blo(kc[0][0]) + blo(dk), bhi(kc[0][0]));
    char* base = smem + L_GRP + gk * GRP;
    const u4 z4 = {0u, 0u, 0u, 0u};
    if (l < 40) *(u4*)(base + (l < 8 ? (l >> 1) * KCS + 16 * (l & 1) : G_U + 4096 + 16 * (l - 8))) = z4;
#pragma unroll
    for (int j = 0; j < 4; ++j) {
      const unsigned o0 = __builtin_amdgcn_alignbit(kp[j][1], kp[j][0], 16), o1 = __builtin_amdgcn_alignbit(kc[j][0], kp[j][1], 16),
                     o2 = __builtin_amdgcn_alignbit(kc[j][1], kc[j][0], 16), o3 = __builtin_amdgcn_alignbit(kc[j][2], kc[j][1], 16),
                     o4 = __builtin_amdgcn_alignbit(kc[j][3], kc[j][2], 16);
      char* kp0 = base + 32 + 16 * l + 1024 * j;
      const u4 c1 = {o1, o2, o3, o4}, c2 = {kp[j][1], kc[j][0], kc[j][1], kc[j][2]}, c3 = {o0, o1, o2, o3};
      *(u4*)(kp0) = kc[j]; *(u4*)(kp0 + KCS) = c1; *(u4*)(kp0 + 2 * KCS) = c2; *(u4*)(kp0 + 3 * KCS) = c3;
    }
    arrive(SY(C_K + gk));
    conv = true;
  }

  if (conv) {
    LANES();
    const int wl = w & 3, t = tid & 255;
    char* base = smem + L_GRP + gi * GRP;
    char* ul = base + G_U;
    const rsrc ru = mkrs(ws + WS_KV + (96 * xcd + 3 * idx + (gi == 2 ? 2 - 2 * (idx & 1) : gi + (idx & 1))) * CP, 4096);
    int ao = (3 - (i >> 2)) * KCS + 16 * g + 8 * (i & 3) + 8 + 128 * wl;
    int bo = G_U + 4064 - 3584 - 32 * i + 16 * g + 128 * wl;
    int wo = wl * 1024 + 16 * (4 * i + ((g + (i >> 1)) & 3));
    const int c = (t >> 1) & 15, r0 = (wl < 2 ? G_P : -16384) + 4096 * (t >> 5) + 64 * c;
    int p0 = r0 + 16 * ((2 * (t & 1) + (c >> 1)) & 3), p1 = r0 + 16 * ((2 * (t & 1) + 1 + (c >> 1)) & 3);
    const int mask = gi < 2 ? 0xff : ((idx & 1) ? 0x3b : 0xc4);
    int yo = ((mask >> (t >> 5)) & 1) ? 4080 - 16 * t : 4096;
    PIN(ao); PIN(bo); PIN(wo); PIN(p0); PIN(p1); PIN(yo);
    if (w < 13) {
      drain();
      arrive(w < 4 ? SY(C_A1D) : SY(C_B1D));
    }
    if (w == 12) {
      const unsigned tag = *(const unsigned*)(smem + SY(C_TAG));
      await<0>(SY(C_B1D), 5);
      if (l == 0) st32<SC1>(tag, rf, FL_B1 + 256 * cgp + 4 * rb, 0);
      pace(tstart, XB, 35);
      gather<false>(rf, FL_B1 + 256 * (xcd >> 1) + 4 * l, tag);
      arrive(SY(C_GOB));
    }
    u4 a0[8];
    await<1>(SY(C_K + gi), 1);
#pragma unroll
    for (int e = 0; e < 8; ++e) { a0[e] = cat(trread(base + ao + 512 * e), trread(base + ao + 512 * e + 8)); PIN(a0[e]); }
    __builtin_amdgcn_s_setprio(3);
    await<1>(SY(C_GOB), 1);
    const u4 uv = ld128<SC1>(ru, 16 * t, 0);
    *(u4*)(ul + 16 * t) = uv;
    const int cg = SY(C_G + gi);
    arrive(cg);
    await<0>(cg, 4);
    if (gi == 2) __builtin_amdgcn_s_setprio(2);
    if (gi == 0) __builtin_amdgcn_s_setprio(1);
    if (gi == 1) __builtin_amdgcn_s_setprio(0);
    if (gi < 2) conv_part<0xff>(base, ao, bo, wo, cg, a0);
    else if (idx & 1) conv_part<0x3b>(base, ao, bo, wo, cg, a0);
    else conv_part<0xc4>(base, ao, bo, wo, cg, a0);
    arrive(cg);
    await<0>(cg, 12);
    f4 pa[4], pb[4];
#pragma unroll
    for (int ww = 0; ww < 4; ++ww) { pa[ww] = *(const f4*)(base + p0 + ww * 1024); pb[ww] = *(const f4*)(base + p1 + ww * 1024); }
    float sm[8], sn[8];
#pragma unroll
    for (int e = 0; e < 4; ++e) { sm[e] = pa[0][e] + pa[1][e]; sm[4 + e] = pb[0][e] + pb[1][e]; sn[e] = pa[2][e] + pa[3][e]; sn[4 + e] = pb[2][e] + pb[3][e]; }
    OPAQUE8(sm); OPAQUE8(sn);
#pragma unroll
    for (int e = 0; e < 8; ++e) sm[e] += sn[e];
    OPAQUE8(sm);
    const u4 o = {pk(sm[7], sm[6]), pk(sm[5], sm[4]), pk(sm[3], sm[2]), pk(sm[1], sm[0])};
    st128<SC1>(o | 1u, ru, yo);
    __builtin_amdgcn_s_setprio(0);
    arrive(SY(C_B2D));
  }

  if (w < 8) {
    LANES();
    u4 C0 = FRAG(F_C0), S0 = FRAG(F_S0), QC = FRAG(F_QC), QS = FRAG(F_QS), QM = FRAG(F_QM);
    f4 tc[3], tn[3];
#pragma unroll
    for (int j = 0; j < 3; ++j) { tc[j] = FRAGF(F_TC + j); tn[j] = FRAGF(F_TS + j); PIN(tc[j]); PIN(tn[j]); }
    u4 N0 = S0 ^ SGN2;
    PIN(C0); PIN(S0); PIN(N0); PIN(QC); PIN(QS); PIN(QM);
    int zo = L_U + 2 * w * ZP + 40 * (8 * h + ((l >> 2) & 3)) + 8 * min(4 * ((l >> 4) & 1) + (l & 3), 4);
    int zw = L_U + 2 * w * ZP + 72 * min(n32, 20) + 8 * h, zs = L_U + 2 * w * ZP + 72 * (l >> 2) + 16 * (l & 3);
    const rsrc ro = mkrs(out + (64 * (8 * q + w) + s2) * 1280, 1280);
    PIN(zo); PIN(zw); PIN(zs);
    asm volatile("" :: "s"(ro));
    if (w < 6) {
      u4 m3[2] = {FRAG(F_M3), FRAG(F_M3 + 1)};
      const rsrc rt = mkrs(ws + WS_T + s2 * TP * 4, TP * 4);
      int vo = 4 * (4 * g * SLOT + (w < 5 ? 64 * w + 4 * i : 320));
      int wo = L_U + 4 * g * ZP + 128 * w + 8 * i;
      PIN(m3[0]); PIN(m3[1]); PIN(vo); PIN(wo);
      await<2>(SY(C_A2D), 3);
      __builtin_amdgcn_s_sleep(D_X2);
      pace(tstart, X2A, 0);
      __builtin_amdgcn_s_setprio(3);
      const f4 z = {0.f, 0.f, 0.f, 0.f};
      bool bad = true;
      if (w < 5) {
        u4 tv[8];
        f4 acc[4];
        SPIN_LOOP {
          CFENCE();
          if (bad) {
#pragma unroll
            for (int j = 0; j < 8; ++j) { const int jr = (j + 2 * q) & 7; tv[j] = ld128<SC1>(rt, vo, 4 * ((jr & 3) + 16 * (jr >> 2)) * SLOT); }
          }
          unsigned m = 1u;
#pragma unroll
          for (int kk = 0; kk < 2; ++kk) {
#pragma unroll
            for (int t = 0; t < 4; ++t) {
              const u4 b = {tv[4 * kk][t], tv[4 * kk + 1][t], tv[4 * kk + 2][t], tv[4 * kk + 3][t]};
              acc[t] = mfma16(m3[kk], b, kk ? acc[t] : z);
              m = and4(m, b);
            }
            SCHED();
          }
          bad = m == 0u;
          if (!__any(bad)) break;
          __builtin_amdgcn_s_sleep(D_RETRY);
        }
#pragma unroll
        for (int r = 0; r < 4; ++r) {
          const u2 o = {pk(acc[0][r], acc[1][r]), pk(acc[2][r], acc[3][r])};
          *(u2*)(smem + wo + r * ZP) = o;
        }
      } else {
        u4 nv[2];
        SPIN_LOOP {
          CFENCE();
#pragma unroll
          for (int j = 0; j < 8; ++j) { const int jr = (j + 2 * q) & 7; nv[j >> 2][j & 3] = ld32<SC1>(rt, vo, 4 * ((jr & 3) + 16 * (jr >> 2)) * SLOT); }
          if (__all(and4(and4(1u, nv[0]), nv[1]) != 0u)) break;
          __builtin_amdgcn_s_sleep(D_RETRY);
        }
        const f4 acc = mfma16(m3[1], nv[1], mfma16(m3[0], nv[0], z));
        *(float*)(smem + wo) = acc[0]; *(float*)(smem + wo + 2 * ZP) = acc[2];
      }
      arrive(SY(C_A3));
    }
    await<0>(SY(C_A3), 6);
    {
      const char* zp = smem + zo;
      const u4 are = cat(trread(zp), trread(zp + 160)), aim = cat(trread(zp + ZP), trread(zp + ZP + 160));
      const float nyq = *(const float*)(smem + L_U + 2 * w * ZP + 640);
      f16v vr = {}, vi = {};
      vr = mfma32(are, C0, vr); vi = mfma32(are, S0, vi);
      vr = mfma32(aim, N0, vr); vi = mfma32(aim, C0, vi);
      f16v y;
#pragma unroll
      for (int r = 0; r < 16; ++r) y[r] = (r & 1) ? -nyq : nyq;
      u4 br, bi, bm;
      twiddle<true>(vr, vi, tc, tn, br, bi, bm);
      y = mfma32(br, QC, y); y = mfma32(bi, QS, y); y = mfma32(bm, QM, y);
#pragma unroll
      for (int t = 0; t < 4; ++t) {
        const u2 o = {pk(y[4 * t], y[4 * t + 1]), pk(y[4 * t + 2], y[4 * t + 3])};
        *(u2*)(smem + zw + 16 * t) = o;
      }
      const char* zq = smem + zs;
      const u4 o0 = cat(*(const u2*)zq, *(const u2*)(zq + 8)), o1 = cat(*(const u2*)(zq + 72 * 16), *(const u2*)(zq + 72 * 16 + 8));
      st128<SC1>(o0, ro, 16 * l);
      st128<SC1>(o1, ro, 16 * l + 1024);
    }
    if (w == 4 && blockIdx.x == 0 && l == 0) st32<SC1>(*(const unsigned*)(smem + SY(C_TAG)), rf, FL_EPOCH, 0);
  } else if (w < 13) {
    LANES();
    const int t = tid - 512;
    constexpr int L_Y = L_GRP;
    u4 yv[2];
    const rsrc ry = mkrs(ws + WS_KV + 192 * cgp * CP + 4032 - 64 * rb, 192 * CP);
    int yi = ((t >> 2) + ((t >> 2) + 2 + ((0x3b >> xcd) & 1)) / 5) * CP + 16 * (t & 3);
    PIN(yi);
    await<2>(SY(C_B2D), 12);
    __builtin_amdgcn_s_sleep(D_X2);
    pace(tstart, X2B, 0);
    __builtin_amdgcn_s_setprio(3);
#pragma unroll
    for (int j = 0; j < 2; ++j) yv[j] = ld128<SC1>(ry, yi, j * 96 * CP);
    SCHED();
    const int c8 = t >> 4;
    await<0>(SY(C_Q), 1);
    f2 qf[2][4];
#pragma unroll
    for (int j = 0; j < 2; ++j) {
      const u4 qv = *(const u4*)(smem + L_IMG + (31 - i - 16 * j) * 320 + 16 * c8);
#pragma unroll
      for (int e = 0; e < 4; ++e) { qf[j][e] = unp(qv[e]); PIN(qf[j][e]); }
    }
    int wo = L_Y + (t >> 2) * 80 + 16 * (t & 3);
    int po = L_Y + (8 * c8 + (i >> 2)) * 80 + 8 * (i & 3), oo = (15 - (l >> 2)) * 2560 + 64 * (w - 8) + 16 * (l & 3);
    int xo = L_Y + 16384 + 2560 * (w - 8) + 80 * i + 16 * g, xi = L_Y + 16384 + 2560 * (w - 8) + 80 * (l >> 2) + 16 * (l & 3);
    const rsrc ro = mkrs(out + 32 * rb * 1280 + 640 + 160 * cgp, 32 * 2560);
    asm volatile("" :: "s"(ro));
    PIN(wo); PIN(po); PIN(oo); PIN(xo); PIN(xi);
    SCHED();
    SPIN_LOOP {
      const bool bad = and4(and4(1u, yv[0]), yv[1]) == 0u;
      if (!__any(bad)) break;
      __builtin_amdgcn_s_sleep(D_RETRY);
      CFENCE();
      if (bad) {
#pragma unroll
        for (int j = 0; j < 2; ++j) yv[j] = ld128<SC1>(ry, yi, j * 96 * CP);
      }
    }
#pragma unroll
    for (int j = 0; j < 2; ++j) *(u4*)(smem + wo + 80 * 80 * j) = yv[j];
    arrive(SY(C_B1));
    await<0>(SY(C_B1), 10);
    u4 o[2];
#pragma unroll
    for (int j = 0; j < 2; ++j) {
      const char* p = smem + po + 32 * j;
      const u4 y = cat(trread(p), trread(p + 4 * 80));
      float v[8];
#pragma unroll
      for (int e = 0; e < 4; ++e) { v[2 * e] = blo(y[e]) * qf[j][e][0]; v[2 * e + 1] = bhi(y[e]) * qf[j][e][1]; }
      OPAQUE8(v);
#pragma unroll
      for (int e = 0; e < 4; ++e) o[j][e] = pk(v[2 * e], v[2 * e + 1]);
      *(u4*)(smem + xo + 1280 * j) = o[j];
    }
#pragma unroll
    for (int j = 0; j < 2; ++j) st128<SC1>(*(const u4*)(smem + xi + 1280 * j), ro, oo + (1 - j) * 16 * 2560);
  }
}

extern "C" __device__ __attribute__((used, noinline)) void gfh3_code_end() {}

extern "C" void kernel_launch(void* const* d_in, const int* in_sizes, int n_in, void* d_out, int out_size, void* d_ws,
                              size_t ws_size, hipStream_t stream) {
  gfh3<<<256, 1024, 0, stream>>>((const uint16_t*)d_in[0], (const uint16_t*)d_in[1], (const uint16_t*)d_in[2], (const uint16_t*)d_in[3],
                                 (const uint16_t*)d_in[4], (const uint16_t*)d_in[5], (const uint16_t*)d_in[6], (const uint16_t*)d_in[7],
                                 (const uint16_t*)d_in[8], (uint16_t*)d_out, (char*)d_ws);
}
```

```cpp
#include <hip/hip_runtime.h>
#include <stdint.h>
#include <utility>

typedef __attribute__((ext_vector_type(2))) __bf16 bf2;
typedef __attribute__((ext_vector_type(8))) __bf16 bf8;
typedef __attribute__((ext_vector_type(2))) float f2;
typedef __attribute__((ext_vector_type(4))) float f4;
typedef __attribute__((ext_vector_type(16))) float f16v;
typedef __attribute__((ext_vector_type(2))) unsigned u2;
typedef __attribute__((ext_vector_type(4))) unsigned u4;
typedef __attribute__((ext_vector_type(4))) short s4;
typedef __amdgpu_buffer_rsrc_t rsrc;
#define DI __device__ __forceinline__
#define LDS __attribute__((address_space(3)))
#define GLB __attribute__((address_space(1)))
#define PIN(x) asm volatile("" : "+v"(x))
#define SCHED() __builtin_amdgcn_sched_barrier(0)
#define BAR() asm volatile("s_barrier" ::: "memory")
#define LBAR() asm volatile("s_waitcnt lgkmcnt(0)\n\ts_barrier" ::: "memory")
#define CFENCE() asm volatile("" ::: "memory")
#define SPIN_LOOP _Pragma("unroll 1") for (unsigned spin = 0; spin < SPIN_MAX; ++spin)

#define XA 490
#define XB 418
#define X2A 768
#define X2B 825
constexpr int D_X2 = 6, D_RETRY = 4;
constexpr int NT = 0;
constexpr int SC1 = 16;
constexpr int NCOL = 32 * 321;
constexpr int SLOT = 352;
constexpr int TP = 32 * SLOT + 32;
constexpr int CP = 4096 + 128;
constexpr unsigned SGN2 = 0x80008000u;
constexpr unsigned SPIN_MAX = 1u << 18;

constexpr int WS_T = 0, WS_KV = 64 * TP * 4, WS_FLAG = WS_KV + 768 * CP, FL_A1 = 0, FL_B1 = 8 * 1024, FL_EPOCH = 9 * 1024;

enum { F_C0, F_S0, F_PC, F_PS, F_MR, F_QC, F_QS, F_QM, F_TC, F_TS = F_TC + 3, F_AR = F_TS + 3, F_M3 = F_AR + 4, F_ALL = F_M3 + 2 };

DI unsigned pk(float a, float b) { f2 v = {a, b}; return __builtin_bit_cast(unsigned, __builtin_convertvector(v, bf2)); }
DI float blo(unsigned v) { return __uint_as_float(v << 16); }
DI float bhi(unsigned v) { return __uint_as_float(v & 0xffff0000u); }
DI f2 unp(unsigned v) { f2 r = {blo(v), bhi(v)}; return r; }
DI float fcos(float r) { return __builtin_amdgcn_cosf(r); }
DI float fsin(float r) { return __builtin_amdgcn_sinf(r); }
DI f4 mfma16(u4 a, u4 b, f4 c) { return __builtin_amdgcn_mfma_f32_16x16x32_bf16(__builtin_bit_cast(bf8, a), __builtin_bit_cast(bf8, b), c, 0, 0, 0); }
DI f16v mfma32(u4 a, u4 b, f16v c) { return __builtin_amdgcn_mfma_f32_32x32x16_bf16(__builtin_bit_cast(bf8, a), __builtin_bit_cast(bf8, b), c, 0, 0, 0); }
DI u2 trread(const char* p) { return __builtin_bit_cast(u2, __builtin_amdgcn_ds_read_tr16_b64_v4i16((LDS s4*)p)); }
DI u4 cat(u2 a, u2 b) { u4 r = {a[0], a[1], b[0], b[1]}; return r; }
DI rsrc mkrs(const void* p, unsigned bytes) { return __builtin_amdgcn_make_buffer_rsrc((void*)p, 0, bytes, 0x00020000); }
template <int AUX = 0> DI u4 ld128(rsrc r, int v, int s) { return __builtin_bit_cast(u4, __builtin_amdgcn_raw_buffer_load_b128(r, v, s, AUX)); }
template <int AUX = 0> DI unsigned ld32(rsrc r, int v, int s) { return __builtin_amdgcn_raw_buffer_load_b32(r, v, s, AUX); }
template <int AUX> DI void st128(u4 x, rsrc r, int v) { __builtin_amdgcn_raw_buffer_store_b128(x, r, v, 0, AUX); }
template <int AUX> DI void st32(unsigned x, rsrc r, int v, int s) { __builtin_amdgcn_raw_buffer_store_b32(x, r, v, s, AUX); }
DI unsigned and4(unsigned m, u4 a) { return m & a[0] & a[1] & a[2] & a[3]; }
DI void drain() { asm volatile("s_waitcnt vmcnt(0)" ::: "memory"); }
template <int H> DI f2 half(const f4& a) { return __builtin_shufflevector(a, a, 2 * H, 2 * H + 1); }
template <int H> DI f2 half(const f16v& a) { return __builtin_shufflevector(a, a, 2 * H, 2 * H + 1); }

DI void arrive(int off) {
  SCHED();
  asm volatile("s_mov_b64 exec, 1\n\tds_add_u32 %0, %1\n\ts_mov_b64 exec, -1" :: "v"(off), "v"(1u) : "memory");
  SCHED();
}
template <int SLEEP> DI void await(int off, int n) {
  unsigned v = 0, s;
  asm volatile("1:\n\tds_read_b32 %0, %2\n\ts_waitcnt lgkmcnt(0)\n\tv_readfirstlane_b32 %1, %0\n\ts_cmp_ge_u32 %1, %3\n\t"
               "s_cbranch_scc1 2f\n\ts_sleep %4\n\ts_branch 1b\n2:" : "+&v"(v), "=&s"(s) : "v"(off), "i"(n), "i"(SLEEP) : "memory", "scc");
}

constexpr int KCS = 4160;
constexpr int G_P = 4 * KCS, G_U = G_P + 16384;
constexpr int GRP = G_U + 4608;
constexpr int ZP = 768;
constexpr int L_SYNC = 0, L_TAB = 128, L_U = L_TAB + F_ALL * 1024, L_IMG = L_U + 12 * 1280, L_GRP = L_IMG + 10240, L_END = L_GRP + 3 * GRP;
enum { C_GOA, C_GOB, C_A1, C_A1D, C_B1, C_B1S, C_B1D, C_K, C_G = C_K + 3, C_A2D = C_G + 3, C_A3, C_B2D, C_PUBA, C_XI, C_Q, C_WL, C_TI, C_TAG = 31 };
#define SY(c) (L_SYNC + 4 * (c))

#define OPAQUE8(a) asm("" : "+v"((a)[0]), "+v"((a)[1]), "+v"((a)[2]), "+v"((a)[3]), "+v"((a)[4]), "+v"((a)[5]), "+v"((a)[6]), "+v"((a)[7]))
template <typename F> DI void dft64(const u4 (&ar)[4], const u4 (&ai)[4], const u4 (&b)[4], F f) {
  f4 er[4], ei[4];
  const f4 z = {0.f, 0.f, 0.f, 0.f};
#pragma unroll
  for (int c = 0; c < 4; ++c) { const int cc = (c >> 1) | ((c & 1) << 1); er[cc] = mfma16(ar[cc], b[cc], z); ei[cc] = mfma16(ai[cc], b[cc], z); }
#pragma unroll
  for (int rr = 0; rr < 4; ++rr) {
    const int r = (rr >> 1) | ((rr & 1) << 1);
    float t[8] = {er[0][r] + er[2][r], ei[0][r] + ei[2][r], er[0][r] - er[2][r], ei[0][r] - ei[2][r],
                  er[1][r] + er[3][r], ei[1][r] + ei[3][r], er[1][r] - er[3][r], ei[1][r] - ei[3][r]};
    OPAQUE8(t);
    float x[8] = {t[0] + t[4], t[1] + t[5], t[2] + t[7], t[3] - t[6], t[0] - t[4], t[1] - t[5], t[2] - t[7], t[3] + t[6]};
    OPAQUE8(x);
    f(r, x);
  }
}

template <typename F, int... I> DI void static_for_(F f, std::integer_sequence<int, I...>) { (f(std::integral_constant<int, I>{}), ...); }
template <int N, typename F> DI void static_for(F f) { static_for_(f, std::make_integer_sequence<int, N>{}); }
template <bool INV> DI void twiddle(const f16v& vr, const f16v& vi, const f4 (&tc)[3], const f4 (&tn)[3], u4& br, u4& bi, u4& bm) {
  unsigned pr[6], pi[6];
  f2 re[6], im[6];
  static_for<6>([&](auto jc) {
    constexpr int J = decltype(jc)::value;
    re[J] = half<J>(vi) * half<J & 1>(tn[J >> 1]); im[J] = half<J>(vr) * half<J & 1>(tn[J >> 1]);
  });
  SCHED();
  static_for<6>([&](auto jc) {
    constexpr int J = decltype(jc)::value;
    re[J] = __builtin_elementwise_fma(half<J>(vr), half<J & 1>(tc[J >> 1]), INV ? -re[J] : re[J]);
    im[J] = __builtin_elementwise_fma(half<J>(vi), half<J & 1>(tc[J >> 1]), INV ? im[J] : -im[J]);
  });
  SCHED();
#pragma unroll
  for (int j = 0; j < 6; ++j) { pr[j] = pk(re[j][0], re[j][1]); pi[j] = pk(im[j][0], im[j][1]); }
  br = (u4){pr[0], pr[1], pr[2], pr[3]}; bi = (u4){pi[0], pi[1], pi[2], pi[3]}; bm = (u4){pr[4], pr[5], pi[4], pi[5]};
}

struct ConvList { int n, a[36], b[36]; };
constexpr ConvList conv_list(int mask) {
  ConvList c{};
  for (int s = 0; s < 8; ++s) {
    for (int e = 0; e <= s; ++e) if (s + e < 8 && ((mask >> (s + e)) & 1)) { c.a[c.n] = s; c.b[c.n] = e; ++c.n; }
    for (int e = 0; e < s; ++e) if (s + e < 8 && ((mask >> (s + e)) & 1)) { c.a[c.n] = e; c.b[c.n] = s; ++c.n; }
  }
  return c;
}
constexpr int conv_rd(int n) {
  constexpr int RB[5] = {0, 4, 10, 14, 17};
  for (int f = 0; f < 5; ++f) if (n == RB[f]) return 3 * (3 + f);
  constexpr int RF[8] = {21, 24, 27, 32, 36, 39, 42, 45};
  for (int f = 0; f < 8; ++f) if (n >= RF[f] && n < RF[f] + 3) return 3 * (8 + f) + n - RF[f];
  return -1;
}
constexpr int conv_wr(int n) {
  constexpr int WF[7] = {49, 51, 53, 57, 63, 68, 71};
  for (int j = 0; j < 7; ++j) if (n == WF[j]) return j;
  return -1;
}
template <int MASK> DI void conv_part(char* base, int ao, int bo, int wo, int cg, const u4 (&a0)[8]) {
  constexpr int NB = (MASK & 0x80) ? 8 : 6;
  constexpr ConvList L = conv_list(MASK);
  constexpr bool FULL = MASK == 0xff;
  constexpr int PRO = FULL ? 3 : 4;
  f4 acc[8];
#pragma unroll
  for (int j = 0; j < 8; ++j) acc[j] = (f4){0.f, 0.f, 0.f, 0.f};
  u4 b[2 * NB]; u2 al[NB], ah[NB];
  auto rd = [&](int f, int part) {
    const int rr = f / NB, e = f % NB;
    if (part == 0) b[f] = *(const u4*)(base + bo + 512 * (7 - e) + 64 * rr);
    if (part == 1) al[e] = trread(base + ao + 512 * e + 64);
    if (part == 2) ah[e] = trread(base + ao + 512 * e + 64 + 8);
  };
  auto wr = [&](int j) { if ((MASK >> j) & 1) *(f4*)(base + wo + (j < 4 ? G_P + 4096 * j : 4096 * (j - 4))) = acc[j]; };
#pragma unroll
  for (int f = 0; f < PRO; ++f) { rd(f, 0); SCHED(); }
  constexpr int R1 = 9;
  constexpr int LASTRD = FULL ? 47 : R1 + (3 * NB - 1) / 2;
  unsigned seen = 0;
  static_for<2 * L.n>([&](auto nc) {
    constexpr int n = decltype(nc)::value, o = n < L.n ? 0 : NB, fa = L.a[n % L.n], fb = L.b[n % L.n];
    acc[fa + fb] = mfma16(n < L.n ? a0[fa] : cat(al[fa], ah[fa]), b[o + fb], acc[fa + fb]);
    SCHED();
    if constexpr (FULL) {
      constexpr int f = conv_rd(n), j = conv_wr(n);
      if constexpr (f >= 0) rd(f / 3, f % 3);
      if constexpr (n == 58) seen = *(volatile LDS unsigned*)(size_t)cg;
      if constexpr (j == 4) { if (__builtin_amdgcn_readfirstlane(seen) < 8) await<0>(cg, 8); }
      if constexpr (j >= 0) wr(j);
    } else {
      if constexpr (n < NB - PRO) rd(PRO + n, 0);
      constexpr int k = 2 * (n - R1);
      if constexpr (k >= 0 && k < 3 * NB) { rd(NB + k / 3, k % 3); rd(NB + (k + 1) / 3, (k + 1) % 3); }
    }
    if constexpr (n == LASTRD + 1) arrive(cg);
    SCHED();
  });
  if (FULL) wr(7);
  else {
    await<0>(cg, 8);
#pragma unroll
    for (int j = 0; j < 8; ++j) wr(j);
  }
}

template <bool STREAM> DI void glds16(const void* src, int dst) {
  unsigned keep;
  if (STREAM) asm volatile("s_mov_b32 %0, m0\n\ts_mov_b32 m0, %2\n\ts_nop 0\n\tglobal_load_lds_dwordx4 %1, off nt\n\ts_mov_b32 m0, %0"
                           : "=&s"(keep) : "v"(src), "s"(dst) : "memory");
  else asm volatile("s_mov_b32 %0, m0\n\ts_mov_b32 m0, %2\n\ts_nop 0\n\tglobal_load_lds_dwordx4 %1, off\n\ts_mov_b32 m0, %0"
                    : "=&s"(keep) : "v"(src), "s"(dst) : "memory");
}
DI unsigned now() { return (unsigned)__builtin_amdgcn_s_memrealtime(); }
DI void pace(unsigned t0, int x, int p) {
  const unsigned t1 = now();
  while ((int)(now() - t0) < x || (int)(now() - t1) < p) __builtin_amdgcn_s_sleep(1);
}
template <bool WIDE> DI void gather(rsrc rf, int off, unsigned tag) {
  SPIN_LOOP {
    CFENCE();
    bool ok;
    if (WIDE) { const u4 f = ld128<SC1>(rf, off, 0); ok = f[0] == tag && f[1] == tag && f[2] == tag && f[3] == tag; }
    else ok = ld32<SC1>(rf, off, 0) == tag;
    if (__all(ok)) break;
    __builtin_amdgcn_s_sleep(2);
  }
}

DI int fresh_lane() {
  unsigned z = 0;
  PIN(z);
  return __builtin_amdgcn_mbcnt_hi(~0u, __builtin_amdgcn_mbcnt_lo(~0u, z));
}
#define LANES() const int l = fresh_lane(), tid = 64 * w + l, i = l & 15, g = l >> 4, n32 = l & 31, h = l >> 5; char* tab = smem + L_TAB + 16 * l; \
  (void)tid; (void)i; (void)g; (void)n32; (void)h; (void)tab

extern "C" __global__ __launch_bounds__(1024, 4) void gfh3(const uint16_t* __restrict__ x, const uint16_t* __restrict__ wr, const uint16_t* __restrict__ wi,
                                                        const uint16_t* __restrict__ qin, const uint16_t* __restrict__ kin, const uint16_t* __restrict__ vin,
                                                        const uint16_t* __restrict__ shk, const uint16_t* __restrict__ ssk, const uint16_t* __restrict__ ssd,
                                                        uint16_t* __restrict__ out, char* ws) {
  __shared__ __attribute__((aligned(16))) char smem[L_END];
  const int l = threadIdx.x & 63;
  const int wv0 = (__builtin_amdgcn_readfirstlane(threadIdx.x >> 6) + 8) & 15;
  const int w = wv0 >= 12 ? wv0 ^ 1 : wv0;
  const int tid = 64 * w + l;
  const int xcd = blockIdx.x & 7, idx = blockIdx.x >> 3;
  const int q = idx & 3, s2 = xcd * 8 + (idx >> 2);
  const int rb = 8 * xcd + (idx >> 2), cgp = idx & 3;
  const int i = l & 15, g = l >> 4, n32 = l & 31, h = l >> 5;
  char* tab = smem + L_TAB + 16 * l;
#define FRAG(f) (*(u4*)(tab + 1024 * (f)))
#define FRAGF(f) (*(f4*)(tab + 1024 * (f)))
  const unsigned tstart = now();
  unsigned sink = 0;
  if (w == 8) {
    unsigned long long pc; int rel, len;
    asm volatile("s_getpc_b64 %0\n\ts_mov_b32 %1, gfh3@rel32@lo+4\n\ts_mov_b32 %2, gfh3_code_end-gfh3" : "=s"(pc), "=s"(rel), "=s"(len));
    const char* p = (const char*)(pc + (long long)rel) + min(128 * (8 * l + (idx & 7)), len);
    asm volatile("global_load_dword %0, %1, off" : "+v"(sink) : "v"(p) : "memory");
  }
  asm volatile("" :: "s"(x), "s"(wr), "s"(wi), "s"(qin), "s"(kin), "s"(vin), "s"(shk), "s"(ssk), "s"(ssd), "s"(out), "s"(ws));
  const rsrc rf = mkrs(ws + WS_FLAG, FL_EPOCH + 4);
  const int gi = w < 8 ? 2 : (w - 8) >> 2;
  bool conv = false;

  if (__builtin_expect((unsigned)(w - 8) < 5u, 1)) {
    const int t = tid - 512, c2 = t % 80, tc = t / 80;
    unsigned kr[11], vv[8], epoch;
    u4 wk;
    {
      const rsrc rk = mkrs(kin, 2048 * 1280), rv = mkrs(vin, 2048 * 1280), rs = mkrs(shk, 640 * 8);
      const int vo = (32 * rb + 8 * tc) * 1280 + 320 * cgp + 4 * c2;
      const int vh = (rb | tc) ? vo - 3 * 1280 : 2048 * 1280;
      epoch = ld32<SC1>(rf, w == 8 ? FL_EPOCH : FL_EPOCH + 4, 0);
      SCHED();
#pragma unroll
      for (int j = 0; j < 8; ++j) { kr[3 + j] = ld32<NT>(rk, vo, j * 1280); vv[j] = ld32<NT>(rv, vo, j * 1280); }
      wk = ld128(rs, 1280 * cgp + 16 * c2, 0);
#pragma unroll
      for (int j = 0; j < 3; ++j) kr[j] = ld32<NT>(rk, vh + j * 1280, 0);
      SCHED();
    }
    const rsrc ro = mkrs(ws + WS_KV + 192 * cgp * CP + 4032 - 64 * rb, 191 * CP + 64);
    int so = (t >> 2) * CP + 16 * (t & 3), ri[3];
#pragma unroll
    for (int j = 0; j < 3; ++j) {
      const int r = min((t >> 2) + 80 * j, 191), ch = r - (r + 3) / 6;
      ri[j] = L_IMG + (ch >> 1) * 128 + 16 * ((4 * (ch & 1) + (t & 3) + (ch >> 1)) & 7);
      PIN(ri[j]);
    }
    int wi0 = L_IMG + c2 * 128 + 16 * ((3 - tc + c2) & 7);
    PIN(so); PIN(wi0);
    SCHED();
    asm volatile("" : "+v"(sink), "+v"(epoch));
    if (w == 8) {
      *(unsigned*)(smem + SY(C_TAG)) = epoch * 0x9E3779B1u + 0x7F4A7C15u;
    }
    LBAR();
    __builtin_amdgcn_s_setprio(3);
    {
      const f2 w0 = {blo(wk[0]), blo(wk[2])}, w1 = {bhi(wk[0]), bhi(wk[2])}, w2 = {blo(wk[1]), blo(wk[3])}, w3 = {bhi(wk[1]), bhi(wk[3])};
      f2 kf[11], o[8];
#pragma unroll
      for (int j = 0; j < 11; ++j) kf[j] = unp(kr[j]);
#pragma unroll
      for (int j = 0; j < 8; ++j) o[j] = (kf[j] * w3 + kf[j + 1] * w2 + kf[j + 2] * w1 + kf[j + 3] * w0) * unp(vv[j]);
#pragma unroll
      for (int e = 0; e < 2; ++e) {
        const u4 r = {pk(o[7][e], o[6][e]), pk(o[5][e], o[4][e]), pk(o[3][e], o[2][e]), pk(o[1][e], o[0][e])};
        *(u4*)(smem + (wi0 ^ (64 * e))) = r & ~1u;
      }
    }
    arrive(SY(C_B1));
    await<0>(SY(C_B1), 5);
#pragma unroll
    for (int j = 0; j < 3; ++j) st128<SC1>(*(const u4*)(smem + ri[j]), ro, so + j * 80 * CP);
    arrive(SY(C_B1S));
    conv = true;
  } else if (__builtin_expect(w < 5, 1)) {
    u4 xv[8];
    const rsrc rx = mkrs(x + s2 * 640 + 128 * w, 0x7fffffff);
    const int vx = g * (512 * 1280) + 16 * i;
#pragma unroll
    for (int j = 0; j < 2; ++j) xv[j] = ld128(rx, vx, (j + 2 * q) * 64 * 1280);
    SCHED();
    u4 m1;
    {
      const int wv = i >> 1;
      const float kf = (wv < 4 ? (float)(4 * q + wv + 1) : 0.f) * (1.f / 2048.f);
      const float ph = 0.25f * (float)(i & 1);
#pragma unroll
      for (int jj = 0; jj < 4; ++jj) {
        const float sb = (float)(512 * g + s2 + 128 * ((jj + q) & 3));
        m1[jj] = pk(fcos(fmaf(kf, sb, ph)), fcos(fmaf(kf, sb + 64.f, ph)));
      }
    }
    const int ks1 = w < 4 ? 4 * q + w + 1 : 0;
    const rsrc rt = mkrs(ws + WS_T + s2 * TP * 4, 32 * SLOT * 4);
    const int cs = h ? (16 - ks1) * SLOT + (ks1 ? 0 : TP) : (ks1 + 16) * SLOT;
    int so = 4 * (cs + (h ? (32 - n32) & 31 : n32));
    int sx = 4 * (cs + 320 + n32 + (n32 < 16 ? 0 : TP));
    int upo = L_U + 2 * w * 1280 + 40 * (8 * h + ((l >> 2) & 3)) + 8 * min(4 * ((l >> 4) & 1) + (l & 3), 4);
    int wro = L_U + 4 * g * 1280 + 256 * w + 16 * i;
    PIN(m1); PIN(so); PIN(sx); PIN(upo); PIN(wro);
    BAR();
#pragma unroll
    for (int j = 2; j < 8; ++j) xv[j] = ld128(rx, vx, ((j + 2 * q) & 7) * 64 * 1280);
    arrive(SY(C_XI));
    u4 C0 = FRAG(F_C0), S0 = FRAG(F_S0);
    f4 tc[3], tn[3];
#pragma unroll
    for (int j = 0; j < 3; ++j) { tc[j] = FRAGF(F_TC + j); PIN(tc[j]); }
    const unsigned sg = (n32 & 1) ? SGN2 : 0u;
    u4 N0 = S0 ^ SGN2, C1 = C0 ^ sg, S1 = S0 ^ sg, N1 = N0 ^ sg;
    PIN(C0); PIN(S0); PIN(N0); PIN(C1); PIN(S1); PIN(N1);
    SCHED();
    __builtin_amdgcn_s_setprio(3);
    {
      f4 acc[8];
      u4 b[8];
#pragma unroll
      for (int jj = 0; jj < 4; ++jj) {
#pragma unroll
        for (int t = 0; t < 8; ++t) {
          b[t][jj] = __builtin_amdgcn_perm(xv[2 * jj + 1][t >> 1], xv[2 * jj][t >> 1], (t & 1) ? 0x07060302u : 0x05040100u);
          const f4 z = {0.f, 0.f, 0.f, 0.f};
          if (jj == 3) acc[t] = mfma16(m1, b[t], z);
        }
        SCHED();
      }
      if (g < 3) {
#pragma unroll
        for (int r = 0; r < 4; ++r) {
          u4 o = {pk(acc[0][r], acc[1][r]), pk(acc[2][r], acc[3][r]), pk(acc[4][r], acc[5][r]), pk(acc[6][r], acc[7][r])};
          *(u4*)(smem + wro + r * 1280) = o;
        }
      }
    }
    arrive(SY(C_A1));
    if (w < 4 || q == 0) {
      const u4 PC = FRAG(F_PC), PS = FRAG(F_PS), MR = FRAG(F_MR);
#pragma unroll
      for (int j = 0; j < 3; ++j) tn[j] = FRAGF(F_TS + j);
      await<0>(SY(C_A1), 5);
      const char* up = smem + upo;
      u4 are[2], aim[2];
#pragma unroll
      for (int kk = 0; kk < 2; ++kk) {
        are[kk] = cat(trread(up + 640 * kk), trread(up + 640 * kk + 160));
        aim[kk] = cat(trread(up + 1280 + 640 * kk), trread(up + 1280 + 640 * kk + 160));
      }
      f16v vr = {}, vi = {};
      vr = mfma32(are[0], C0, vr); vi = mfma32(are[0], N0, vi);
      vr = mfma32(aim[0], S0, vr); vi = mfma32(aim[0], C0, vi);
      vr = mfma32(are[1], C1, vr); vi = mfma32(are[1], N1, vi);
      vr = mfma32(aim[1], S1, vr); vi = mfma32(aim[1], C1, vi);
      const u4 PN = PS ^ SGN2, MI = {MR[2] ^ SGN2, MR[3] ^ SGN2, MR[0], MR[1]};
      u4 br, bi, bm;
      twiddle<false>(vr, vi, tc, tn, br, bi, bm);
      f16v xr = {}, xi = {};
      xr = mfma32(PC, br, xr); xi = mfma32(PN, br, xi);
      xr = mfma32(PS, bi, xr); xi = mfma32(PC, bi, xi);
      xr = mfma32(MR, bm, xr); xi = mfma32(MI, bm, xi);
      const unsigned cj = h ? 0x80000000u : 0u;
      unsigned o[10];
#pragma unroll
      for (int r = 0; r < 10; ++r) o[r] = (pk(xr[r], xi[r]) ^ cj) & ~1u;
      const auto sw = __builtin_amdgcn_permlane32_swap(o[9], o[0], false, false);
#pragma unroll
      for (int r = 1; r < 11; ++r) st32<SC1>(l == 32 ? (r < 10 ? o[r - 1] : sw[0] ^ 0x80000000u) : o[r % 10], rt, so + 128 * (r % 10), 0);
      st32<SC1>(h ? o[9] : sw[1] ^ 0x80000000u, rt, sx, 0);
    }
    if (w == 4) {
      LANES();
      const char* qp[10];
#pragma unroll
      for (int j = 0; j < 10; ++j) {
        const int c = l + 64 * j;
        qp[j] = (const char*)(qin + (32 * rb + c / 20) * 640 + 160 * cgp + 8 * (c % 20));
        PIN(qp[j]);
      }
      drain();
      arrive(SY(C_A1D));
      const unsigned tag = *(const unsigned*)(smem + SY(C_TAG));
      await<1>(SY(C_A1D), 5);
      if (l < 8) st32<SC1>(tag, rf, FL_A1 + 1024 * l + 4 * blockIdx.x, 0);
      arrive(SY(C_PUBA));
      await<1>(SY(C_B1D), 5);
      pace(tstart, XA, 35);
      gather<true>(rf, FL_A1 + 1024 * xcd + 16 * l, tag);
      arrive(SY(C_GOA));
      if (idx < 28) await<0>(SY(C_TI), 3);
#pragma unroll
      for (int j = 0; j < 10; ++j) glds16<true>(qp[j], L_IMG + 1024 * j);
      drain();
      arrive(SY(C_Q));
      if (blockIdx.x == 255 && l == 0) st32<SC1>(tag, rf, FL_EPOCH, 0);
    }
    conv = w < 4;
  } else if (w < 8) {
    const bool act = idx < 28;
    const int slot = 4 * xcd + idx / 7, tile = 3 * (idx % 7) + (w - 5);
    if (w < 7) {
      const int ro = (n32 & 3) + 4 * (n32 >> 3);
      const float a20 = w == 5 ? (float)((n32 & 4) ? 19 - ro : ro) * 0.05f : (float)n32 * -0.05f;
      u4 c, s, m;
#pragma unroll
      for (int jj = 0; jj < 4; ++jj) {
        const float a = a20 * (float)(8 * (jj >> 1) + 4 * h + 2 * (jj & 1)), b = a + a20;
        c[jj] = pk(fcos(a), fcos(b)); s[jj] = pk(fsin(a), fsin(b));
      }
#pragma unroll
      for (int jj = 0; jj < 2; ++jj) {
        const float a = a20 * (float)(16 + 2 * jj), b = a + a20;
        m[jj] = h ? 0u : pk(fcos(a), fcos(b)); m[2 + jj] = h ? 0u : pk(fsin(a), fsin(b));
      }
      char* tp = tab + (w - 5) * 3072;
      *(u4*)(tp + 1024 * F_PC) = c; *(u4*)(tp + 1024 * F_PS) = s; *(u4*)(tp + 1024 * F_MR) = m;
    } else {
      const float a64 = (float)i * (1.f / 64.f);
#pragma unroll
      for (int c = 0; c < 4; ++c) {
        u4 v;
#pragma unroll
        for (int u = 0; u < 4; ++u) { const float a = a64 * (float)(16 * u + c + 4 * g); v[u] = pk(fcos(a), fsin(a)); }
        FRAG(F_AR + c) = v;
      }
    }
    LBAR();
    u4 ar[4], ai[4];
#pragma unroll
    for (int c = 0; c < 4; ++c) {
      ar[c] = FRAG(F_AR + c);
#pragma unroll
      for (int u = 0; u < 4; ++u) ai[c][u] = __builtin_amdgcn_alignbit(ar[c][u], ar[c][u], 16) ^ 0x8000u;
      PIN(ar[c]); PIN(ai[c]);
    }
    const rsrc rt = mkrs(ws + WS_T, 64 * TP * 4);
    int vo = 4 * (4 * g * TP + slot * SLOT + 16 * tile + i);
    const int w0 = (slot - 16) * 321 + 48 * (idx % 7);
    const char* gp[5];
#pragma unroll
    for (int j = 0; j < 5; ++j) {
      const int n = min(3 * j + (w - 5), 13), c = l + 64 * (n % 7);
      int o = 2 * ((w0 & ~7) + (c / 7) * NCOL) + 16 * (c % 7);
      o += (o < 0) ? 128 * NCOL : 0;
      gp[j] = (const char*)(n < 7 ? wr : wi) + min(o, 128 * NCOL - 16);
      PIN(gp[j]);
    }
    PIN(vo);
    if (w < 7) {
      int o = 2 * (1284 * xcd - 16 * 321 - 16 + (2 * idx + h) * NCOL) + 128 * n32;
      o += (o < 0) ? 128 * NCOL : 0;
      await<1>(SY(C_B1S), 5);
      const unsigned t0 = ld32(mkrs(w == 5 ? wr : wi, 128 * NCOL), n32 < 22 ? o : 128 * NCOL, 0);
      asm volatile("" :: "v"(t0));
    }
    await<1>(SY(C_PUBA), 1);
    __builtin_amdgcn_s_setprio(3);
    if (act) {
#pragma unroll
      for (int j = 0; j < 5; ++j)
        if (j < 4 || w < 7) glds16<false>(gp[j], L_U + 1024 * (3 * j + (w - 5)));
    }
    drain();
    arrive(SY(C_WL));
    await<0>(SY(C_WL), 3);
    f2 wa[4][2], wb[4][2];
    const int kd = 16 * tile + i;
    const float wsc2 = (kd == 0 || kd == 320) ? 0.5f : 1.f;
#pragma unroll
    for (int u = 0; u < 4; ++u) {
#pragma unroll
      for (int r = 0; r < 4; ++r) {
        const char* p = smem + L_U + 448 * g + 2 * (i + 16 * (w - 5) + (w0 & 7)) + 1792 * u + 112 * r;
        wa[u][r >> 1][r & 1] = blo(*(const uint16_t*)p); wb[u][r >> 1][r & 1] = blo(*(const uint16_t*)(p + 7168));
      }
      PIN(wa[u][0]); PIN(wb[u][0]); PIN(wa[u][1]); PIN(wb[u][1]);
    }
    if (tile == 0 || tile == 20) {
#pragma unroll
      for (int e = 0; e < 16; ++e) {
        float ta = wa[e >> 2][(e >> 1) & 1][e & 1] * wsc2, tb = wb[e >> 2][(e >> 1) & 1][e & 1] * wsc2;
        asm("" : "+v"(ta), "+v"(tb));
        wa[e >> 2][(e >> 1) & 1][e & 1] = ta; wb[e >> 2][(e >> 1) & 1][e & 1] = tb;
      }
    }
    await<0>(SY(C_GOA), 1);
    if (act) {
      u4 b[4];
#pragma unroll
      for (int c = 0; c < 4; ++c) {
        const int cc = (c >> 1) | ((c & 1) << 1);
#pragma unroll
        for (int u = 0; u < 4; ++u) b[cc][u] = ld32<SC1>(rt, vo, (16 * u + cc) * TP * 4);
      }
      SCHED();
      arrive(SY(C_TI));
      SCHED();
      u4 b2[4];
      dft64(ar, ai, b, [&](int r, float (&x)[8]) {
        float y[8];
#pragma unroll
        for (int u = 0; u < 4; ++u) {
          const float a = wa[u][r >> 1][r & 1], c = wb[u][r >> 1][r & 1];
          y[2 * u] = fmaf(x[2 * u], a, -(x[2 * u + 1] * c)); y[2 * u + 1] = fmaf(-x[2 * u], c, -(x[2 * u + 1] * a));
        }
        OPAQUE8(y);
#pragma unroll
        for (int u = 0; u < 4; ++u) b2[r][u] = pk(y[2 * u], y[2 * u + 1]);
      });
      dft64(ar, ai, b2, [&](int r, float (&x)[8]) {
#pragma unroll
        for (int u = 0; u < 4; ++u) st32<SC1>(pk(x[2 * u], x[2 * u + 1]) | 1u, rt, vo, (16 * u + r) * TP * 4);
      });
    } else {
      __builtin_amdgcn_s_sleep(40);
    }
    __builtin_amdgcn_s_setprio(0);
    arrive(SY(C_A2D));
  } else {
    if (w == 15) {
      if (l < 31) *(unsigned*)(smem + L_SYNC + 4 * l) = 0u;
      unsigned zero = 0;
      PIN(zero);
      if (l == 0 && blockIdx.x < 72)
        __hip_atomic_fetch_or((GLB unsigned*)(ws + WS_FLAG + 128 * blockIdx.x), zero, __ATOMIC_RELAXED, __HIP_MEMORY_SCOPE_AGENT);
    }
    if (w == 13) {
      const float kf = (float)n32, a32 = kf * (1.f / 32.f), a640 = kf * (1.f / 640.f);
      u4 c, s;
#pragma unroll
      for (int jj = 0; jj < 4; ++jj) {
        const float a = a32 * (float)(8 * h + 2 * jj), b = a + a32;
        c[jj] = pk(fcos(a), fcos(b)); s[jj] = pk(fsin(a), fsin(b));
      }
      FRAG(F_C0) = c; FRAG(F_S0) = s;
#pragma unroll
      for (int rg = 0; rg < 3; ++rg) {
        f4 tc, tn;
#pragma unroll
        for (int jj = 0; jj < 4; ++jj) { const float a = a640 * (float)(jj + 8 * rg + 4 * h); tc[jj] = fcos(a); tn[jj] = fsin(a); }
        FRAGF(F_TC + rg) = tc; FRAGF(F_TS + rg) = tn;
      }
    }
    if (w == 14) {
      u4 m3[2];
      const float scale = 2.f / (2048.f * 640.f);
      const float sf = (float)(64 * (8 * q + (i >> 1)) + s2) * (1.f / 2048.f);
      const float ph = -0.25f * (float)(i & 1);
#pragma unroll
      for (int j = 0; j < 8; ++j) {
        const int jr = (j + 2 * q) & 7;
        const float a = fmaf(sf, (float)(16 * (jr >> 2) + (jr & 3) - 16 + 4 * g), ph);
        m3[j >> 2][j & 3] = pk(fcos(a) * scale, fsin(a) * scale);
      }
      FRAG(F_M3) = m3[0]; FRAG(F_M3 + 1) = m3[1];
    }
    LBAR();
    await<0>(SY(C_XI), 5);
    const int gk = w - 13, ck = 80 * xcd + 5 * (idx >> 1) + (gk == 2 ? 2 : gk + 3 * (idx & 1));
    const rsrc rk = mkrs(ssk + ck * 2048, 4096);
    const unsigned dk = __builtin_amdgcn_raw_buffer_load_b16(mkrs(ssd, 1280), 2 * ck, 0, 0);
    u4 kc[4]; u2 kp[4];
#pragma unroll
    for (int j = 0; j < 4; ++j) kc[j] = ld128<NT>(rk, 16 * l + 1024 * j, 0);
    SCHED();
#pragma unroll
    for (int j = 0; j < 4; ++j)
#pragma unroll
      for (int e = 0; e < 2; ++e) {
        const unsigned up = __builtin_amdgcn_update_dpp(0u, kc[j][2 + e], 0x138, 0xf, 0xf, false);
        kp[j][e] = l ? up : (j ? __builtin_amdgcn_readlane(kc[j - 1][2 + e], 63) : 0u);
      }
    if (l == 0) kc[0][0] = pk(blo(kc[0][0]) + blo(dk), bhi(kc[0][0]));
    char* base = smem + L_GRP + gk * GRP;
    const u4 z4 = {0u, 0u, 0u, 0u};
    if (l < 40) *(u4*)(base + (l < 8 ? (l >> 1) * KCS + 16 * (l & 1) : G_U + 4096 + 16 * (l - 8))) = z4;
#pragma unroll
    for (int j = 0; j < 4; ++j) {
      const unsigned o0 = __builtin_amdgcn_alignbit(kp[j][1], kp[j][0], 16), o1 = __builtin_amdgcn_alignbit(kc[j][0], kp[j][1], 16),
                     o2 = __builtin_amdgcn_alignbit(kc[j][1], kc[j][0], 16), o3 = __builtin_amdgcn_alignbit(kc[j][2], kc[j][1], 16),
                     o4 = __builtin_amdgcn_alignbit(kc[j][3], kc[j][2], 16);
      char* kp0 = base + 32 + 16 * l + 1024 * j;
      const u4 c1 = {o1, o2, o3, o4}, c2 = {kp[j][1], kc[j][0], kc[j][1], kc[j][2]}, c3 = {o0, o1, o2, o3};
      *(u4*)(kp0) = kc[j]; *(u4*)(kp0 + KCS) = c1; *(u4*)(kp0 + 2 * KCS) = c2; *(u4*)(kp0 + 3 * KCS) = c3;
    }
    arrive(SY(C_K + gk));
    conv = true;
  }

  if (conv) {
    LANES();
    const int wl = w & 3, t = tid & 255;
    char* base = smem + L_GRP + gi * GRP;
    char* ul = base + G_U;
    const rsrc ru = mkrs(ws + WS_KV + (96 * xcd + 3 * idx + (gi == 2 ? 2 - 2 * (idx & 1) : gi + (idx & 1))) * CP, 4096);
    int ao = (3 - (i >> 2)) * KCS + 16 * g + 8 * (i & 3) + 8 + 128 * wl;
    int bo = G_U + 4064 - 3584 - 32 * i + 16 * g + 128 * wl;
    int wo = wl * 1024 + 16 * (4 * i + ((g + (i >> 1)) & 3));
    const int c = (t >> 1) & 15, r0 = (wl < 2 ? G_P : -16384) + 4096 * (t >> 5) + 64 * c;
    int p0 = r0 + 16 * ((2 * (t & 1) + (c >> 1)) & 3), p1 = r0 + 16 * ((2 * (t & 1) + 1 + (c >> 1)) & 3);
    const int mask = gi < 2 ? 0xff : ((idx & 1) ? 0x3b : 0xc4);
    int yo = ((mask >> (t >> 5)) & 1) ? 4080 - 16 * t : 4096;
    PIN(ao); PIN(bo); PIN(wo); PIN(p0); PIN(p1); PIN(yo);
    if (w < 13) {
      drain();
      arrive(w < 4 ? SY(C_A1D) : SY(C_B1D));
    }
    if (w == 12) {
      const unsigned tag = *(const unsigned*)(smem + SY(C_TAG));
      await<0>(SY(C_B1D), 5);
      if (l == 0) st32<SC1>(tag, rf, FL_B1 + 256 * cgp + 4 * rb, 0);
      pace(tstart, XB, 35);
      gather<false>(rf, FL_B1 + 256 * (xcd >> 1) + 4 * l, tag);
      arrive(SY(C_GOB));
    }
    u4 a0[8];
    await<1>(SY(C_K + gi), 1);
#pragma unroll
    for (int e = 0; e < 8; ++e) { a0[e] = cat(trread(base + ao + 512 * e), trread(base + ao + 512 * e + 8)); PIN(a0[e]); }
    __builtin_amdgcn_s_setprio(3);
    await<1>(SY(C_GOB), 1);
    const u4 uv = ld128<SC1>(ru, 16 * t, 0);
    *(u4*)(ul + 16 * t) = uv;
    const int cg = SY(C_G + gi);
    arrive(cg);
    await<0>(cg, 4);
    if (gi == 2) __builtin_amdgcn_s_setprio(2);
    if (gi == 0) __builtin_amdgcn_s_setprio(1);
    if (gi == 1) __builtin_amdgcn_s_setprio(0);
    if (gi < 2) conv_part<0xff>(base, ao, bo, wo, cg, a0);
    else if (idx & 1) conv_part<0x3b>(base, ao, bo, wo, cg, a0);
    else conv_part<0xc4>(base, ao, bo, wo, cg, a0);
    arrive(cg);
    await<0>(cg, 12);
    f4 pa[4], pb[4];
#pragma unroll
    for (int ww = 0; ww < 4; ++ww) { pa[ww] = *(const f4*)(base + p0 + ww * 1024); pb[ww] = *(const f4*)(base + p1 + ww * 1024); }
    float sm[8], sn[8];
#pragma unroll
    for (int e = 0; e < 4; ++e) { sm[e] = pa[0][e] + pa[1][e]; sm[4 + e] = pb[0][e] + pb[1][e]; sn[e] = pa[2][e] + pa[3][e]; sn[4 + e] = pb[2][e] + pb[3][e]; }
    OPAQUE8(sm); OPAQUE8(sn);
#pragma unroll
    for (int e = 0; e < 8; ++e) sm[e] += sn[e];
    OPAQUE8(sm);
    const u4 o = {pk(sm[7], sm[6]), pk(sm[5], sm[4]), pk(sm[3], sm[2]), pk(sm[1], sm[0])};
    st128<SC1>(o | 1u, ru, yo);
    __builtin_amdgcn_s_setprio(0);
    arrive(SY(C_B2D));
  }

  if (w < 8) {
    LANES();
    u4 C0 = FRAG(F_C0), S0 = FRAG(F_S0), QC = FRAG(F_QC), QS = FRAG(F_QS), QM = FRAG(F_QM);
    f4 tc[3], tn[3];
#pragma unroll
    for (int j = 0; j < 3; ++j) { tc[j] = FRAGF(F_TC + j); tn[j] = FRAGF(F_TS + j); PIN(tc[j]); PIN(tn[j]); }
    u4 N0 = S0 ^ SGN2;
    PIN(C0); PIN(S0); PIN(N0); PIN(QC); PIN(QS); PIN(QM);
    int zo = L_U + 2 * w * ZP + 40 * (8 * h + ((l >> 2) & 3)) + 8 * min(4 * ((l >> 4) & 1) + (l & 3), 4);
    int zw = L_U + 2 * w * ZP + 72 * min(n32, 20) + 8 * h, zs = L_U + 2 * w * ZP + 72 * (l >> 2) + 16 * (l & 3);
    const rsrc ro = mkrs(out + (64 * (8 * q + w) + s2) * 1280, 1280);
    PIN(zo); PIN(zw); PIN(zs);
    asm volatile("" :: "s"(ro));
    if (w < 6) {
      u4 m3[2] = {FRAG(F_M3), FRAG(F_M3 + 1)};
      const rsrc rt = mkrs(ws + WS_T + s2 * TP * 4, TP * 4);
      int vo = 4 * (4 * g * SLOT + (w < 5 ? 64 * w + 4 * i : 320));
      int wo = L_U + 4 * g * ZP + 128 * w + 8 * i;
      PIN(m3[0]); PIN(m3[1]); PIN(vo); PIN(wo);
      await<2>(SY(C_A2D), 3);
      __builtin_amdgcn_s_sleep(D_X2);
      pace(tstart, X2A, 0);
      __builtin_amdgcn_s_setprio(3);
      const f4 z = {0.f, 0.f, 0.f, 0.f};
      bool bad = true;
      if (w < 5) {
        u4 tv[8];
        f4 acc[4];
        SPIN_LOOP {
          CFENCE();
          if (bad) {
#pragma unroll
            for (int j = 0; j < 8; ++j) { const int jr = (j + 2 * q) & 7; tv[j] = ld128<SC1>(rt, vo, 4 * ((jr & 3) + 16 * (jr >> 2)) * SLOT); }
          }
          unsigned m = 1u;
#pragma unroll
          for (int kk = 0; kk < 2; ++kk) {
#pragma unroll
            for (int t = 0; t < 4; ++t) {
              const u4 b = {tv[4 * kk][t], tv[4 * kk + 1][t], tv[4 * kk + 2][t], tv[4 * kk + 3][t]};
              acc[t] = mfma16(m3[kk], b, kk ? acc[t] : z);
              m = and4(m, b);
            }
            SCHED();
          }
          bad = m == 0u;
          if (!__any(bad)) break;
          __builtin_amdgcn_s_sleep(D_RETRY);
        }
#pragma unroll
        for (int r = 0; r < 4; ++r) {
          const u2 o = {pk(acc[0][r], acc[1][r]), pk(acc[2][r], acc[3][r])};
          *(u2*)(smem + wo + r * ZP) = o;
        }
      } else {
        u4 nv[2];
        SPIN_LOOP {
          CFENCE();
#pragma unroll
          for (int j = 0; j < 8; ++j) { const int jr = (j + 2 * q) & 7; nv[j >> 2][j & 3] = ld32<SC1>(rt, vo, 4 * ((jr & 3) + 16 * (jr >> 2)) * SLOT); }
          if (__all(and4(and4(1u, nv[0]), nv[1]) != 0u)) break;
          __builtin_amdgcn_s_sleep(D_RETRY);
        }
        const f4 acc = mfma16(m3[1], nv[1], mfma16(m3[0], nv[0], z));
        *(float*)(smem + wo) = acc[0]; *(float*)(smem + wo + 2 * ZP) = acc[2];
      }
      arrive(SY(C_A3));
    }
    await<0>(SY(C_A3), 6);
    {
      const char* zp = smem + zo;
      const u4 are = cat(trread(zp), trread(zp + 160)), aim = cat(trread(zp + ZP), trread(zp + ZP + 160));
      const float nyq = *(const float*)(smem + L_U + 2 * w * ZP + 640);
      f16v vr = {}, vi = {};
      vr = mfma32(are, C0, vr); vi = mfma32(are, S0, vi);
      vr = mfma32(aim, N0, vr); vi = mfma32(aim, C0, vi);
      f16v y;
#pragma unroll
      for (int r = 0; r < 16; ++r) y[r] = (r & 1) ? -nyq : nyq;
      u4 br, bi, bm;
      twiddle<true>(vr, vi, tc, tn, br, bi, bm);
      y = mfma32(br, QC, y); y = mfma32(bi, QS, y); y = mfma32(bm, QM, y);
#pragma unroll
      for (int t = 0; t < 4; ++t) {
        const u2 o = {pk(y[4 * t], y[4 * t + 1]), pk(y[4 * t + 2], y[4 * t + 3])};
        *(u2*)(smem + zw + 16 * t) = o;
      }
      CFENCE();
      const char* zq = smem + zs;
      const u4 o0 = cat(*(const u2*)zq, *(const u2*)(zq + 8)), o1 = cat(*(const u2*)(zq + 72 * 16), *(const u2*)(zq + 72 * 16 + 8));
      st128<SC1>(o0, ro, 16 * l);
      st128<SC1>(o1, ro, 16 * l + 1024);
    }
  } else if (w < 13) {
    LANES();
    const int t = tid - 512;
    constexpr int L_Y = L_GRP;
    u4 yv[2];
    const rsrc ry = mkrs(ws + WS_KV + 192 * cgp * CP + 4032 - 64 * rb, 192 * CP);
    int yi = ((t >> 2) + ((t >> 2) + 2 + ((0x3b >> xcd) & 1)) / 5) * CP + 16 * (t & 3);
    PIN(yi);
    await<2>(SY(C_B2D), 12);
    __builtin_amdgcn_s_sleep(D_X2);
    pace(tstart, X2B, 0);
    __builtin_amdgcn_s_setprio(3);
#pragma unroll
    for (int j = 0; j < 2; ++j) yv[j] = ld128<SC1>(ry, yi, j * 96 * CP);
    SCHED();
    const int c8 = t >> 4;
    await<0>(SY(C_Q), 1);
    f2 qf[2][4];
#pragma unroll
    for (int j = 0; j < 2; ++j) {
      const u4 qv = *(const u4*)(smem + L_IMG + (31 - i - 16 * j) * 320 + 16 * c8);
#pragma unroll
      for (int e = 0; e < 4; ++e) { qf[j][e] = unp(qv[e]); PIN(qf[j][e]); }
    }
    int wo = L_Y + (t >> 2) * 80 + 16 * (t & 3);
    int po = L_Y + (8 * c8 + (i >> 2)) * 80 + 8 * (i & 3), oo = (15 - (l >> 2)) * 2560 + 64 * (w - 8) + 16 * (l & 3);
    int xo = L_Y + 16384 + 2560 * (w - 8) + 80 * i + 16 * g, xi = L_Y + 16384 + 2560 * (w - 8) + 80 * (l >> 2) + 16 * (l & 3);
    const rsrc ro = mkrs(out + 32 * rb * 1280 + 640 + 160 * cgp, 32 * 2560);
    asm volatile("" :: "s"(ro));
    PIN(wo); PIN(po); PIN(oo); PIN(xo); PIN(xi);
    SCHED();
    SPIN_LOOP {
      const bool bad = and4(and4(1u, yv[0]), yv[1]) == 0u;
      if (!__any(bad)) break;
      __builtin_amdgcn_s_sleep(D_RETRY);
      CFENCE();
      if (bad) {
#pragma unroll
        for (int j = 0; j < 2; ++j) yv[j] = ld128<SC1>(ry, yi, j * 96 * CP);
      }
    }
#pragma unroll
    for (int j = 0; j < 2; ++j) *(u4*)(smem + wo + 80 * 80 * j) = yv[j];
    arrive(SY(C_B1));
    await<0>(SY(C_B1), 10);
    u4 o[2];
#pragma unroll
    for (int j = 0; j < 2; ++j) {
      const char* p = smem + po + 32 * j;
      const u4 y = cat(trread(p), trread(p + 4 * 80));
      float v[8];
#pragma unroll
      for (int e = 0; e < 4; ++e) { v[2 * e] = blo(y[e]) * qf[j][e][0]; v[2 * e + 1] = bhi(y[e]) * qf[j][e][1]; }
      OPAQUE8(v);
#pragma unroll
      for (int e = 0; e < 4; ++e) o[j][e] = pk(v[2 * e], v[2 * e + 1]);
      *(u4*)(smem + xo + 1280 * j) = o[j];
    }
    CFENCE();
#pragma unroll
    for (int j = 0; j < 2; ++j) st128<SC1>(*(const u4*)(smem + xi + 1280 * j), ro, oo + (1 - j) * 16 * 2560);
  }
}

extern "C" __device__ __attribute__((used, noinline)) void gfh3_code_end() {}

extern "C" void kernel_launch(void* const* d_in, const int* in_sizes, int n_in, void* d_out, int out_size, void* d_ws,
                              size_t ws_size, hipStream_t stream) {
  gfh3<<<256, 1024, 0, stream>>>((const uint16_t*)d_in[0], (const uint16_t*)d_in[1], (const uint16_t*)d_in[2], (const uint16_t*)d_in[3],
                                 (const uint16_t*)d_in[4], (const uint16_t*)d_in[5], (const uint16_t*)d_in[6], (const uint16_t*)d_in[7],
                                 (const uint16_t*)d_in[8], (uint16_t*)d_out, (char*)d_ws);
}
```

```cpp
#include <hip/hip_runtime.h>
#include <stdint.h>
#include <utility>

typedef __attribute__((ext_vector_type(2))) __bf16 bf2;
typedef __attribute__((ext_vector_type(8))) __bf16 bf8;
typedef __attribute__((ext_vector_type(2))) float f2;
typedef __attribute__((ext_vector_type(4))) float f4;
typedef __attribute__((ext_vector_type(16))) float f16v;
typedef __attribute__((ext_vector_type(2))) unsigned u2;
typedef __attribute__((ext_vector_type(4))) unsigned u4;
typedef __attribute__((ext_vector_type(4))) short s4;
typedef __amdgpu_buffer_rsrc_t rsrc;
#define DI __device__ __forceinline__
#define LDS __attribute__((address_space(3)))
#define GLB __attribute__((address_space(1)))
#define PIN(x) asm volatile("" : "+v"(x))
#define SCHED() __builtin_amdgcn_sched_barrier(0)
#define BAR() asm volatile("s_barrier" ::: "memory")
#define LBAR() asm volatile("s_waitcnt lgkmcnt(0)\n\ts_barrier" ::: "memory")
#define CFENCE() asm volatile("" ::: "memory")
#define SPIN_LOOP _Pragma("unroll 1") for (unsigned spin = 0; spin < SPIN_MAX; ++spin)

#define XA 495
#define XB 422
#define X2A 773
#define X2B 829
constexpr int D_X2 = 6, D_RETRY = 4;
constexpr int NT = 2;
constexpr int SC1 = 16;
constexpr int NCOL = 32 * 321;
constexpr int SLOT = 352;
constexpr int TP = 32 * SLOT + 32;
constexpr int CP = 4096 + 128;
constexpr unsigned SGN2 = 0x80008000u;
constexpr unsigned SPIN_MAX = 1u << 18;

constexpr int WS_T = 0, WS_KV = 64 * TP * 4, WS_FLAG = WS_KV + 768 * CP, FL_A1 = 0, FL_B1 = 8 * 1024, FL_EPOCH = 9 * 1024;

enum { F_C0, F_S0, F_PC, F_PS, F_MR, F_QC, F_QS, F_QM, F_TC, F_TS = F_TC + 3, F_AR = F_TS + 3, F_M3 = F_AR + 4, F_ALL = F_M3 + 2 };

DI unsigned pk(float a, float b) { f2 v = {a, b}; return __builtin_bit_cast(unsigned, __builtin_convertvector(v, bf2)); }
DI float blo(unsigned v) { return __uint_as_float(v << 16); }
DI float bhi(unsigned v) { return __uint_as_float(v & 0xffff0000u); }
DI f2 unp(unsigned v) { f2 r = {blo(v), bhi(v)}; return r; }
DI float fcos(float r) { return __builtin_amdgcn_cosf(r); }
DI float fsin(float r) { return __builtin_amdgcn_sinf(r); }
DI f4 mfma16(u4 a, u4 b, f4 c) { return __builtin_amdgcn_mfma_f32_16x16x32_bf16(__builtin_bit_cast(bf8, a), __builtin_bit_cast(bf8, b), c, 0, 0, 0); }
DI f16v mfma32(u4 a, u4 b, f16v c) { return __builtin_amdgcn_mfma_f32_32x32x16_bf16(__builtin_bit_cast(bf8, a), __builtin_bit_cast(bf8, b), c, 0, 0, 0); }
DI u2 trread(const char* p) { return __builtin_bit_cast(u2, __builtin_amdgcn_ds_read_tr16_b64_v4i16((LDS s4*)p)); }
DI u4 cat(u2 a, u2 b) { u4 r = {a[0], a[1], b[0], b[1]}; return r; }
DI rsrc mkrs(const void* p, unsigned bytes) { return __builtin_amdgcn_make_buffer_rsrc((void*)p, 0, bytes, 0x00020000); }
template <int AUX = 0> DI u4 ld128(rsrc r, int v, int s) { return __builtin_bit_cast(u4, __builtin_amdgcn_raw_buffer_load_b128(r, v, s, AUX)); }
template <int AUX = 0> DI unsigned ld32(rsrc r, int v, int s) { return __builtin_amdgcn_raw_buffer_load_b32(r, v, s, AUX); }
template <int AUX> DI void st128(u4 x, rsrc r, int v) { __builtin_amdgcn_raw_buffer_store_b128(x, r, v, 0, AUX); }
template <int AUX> DI void st32(unsigned x, rsrc r, int v, int s) { __builtin_amdgcn_raw_buffer_store_b32(x, r, v, s, AUX); }
DI unsigned and4(unsigned m, u4 a) { return m & a[0] & a[1] & a[2] & a[3]; }
DI void drain() { asm volatile("s_waitcnt vmcnt(0)" ::: "memory"); }
template <int H> DI f2 half(const f4& a) { return __builtin_shufflevector(a, a, 2 * H, 2 * H + 1); }
template <int H> DI f2 half(const f16v& a) { return __builtin_shufflevector(a, a, 2 * H, 2 * H + 1); }

DI void arrive(int off) {
  SCHED();
  asm volatile("s_mov_b64 exec, 1\n\tds_add_u32 %0, %1\n\ts_mov_b64 exec, -1" :: "v"(off), "v"(1u) : "memory");
  SCHED();
}
template <int SLEEP> DI void await(int off, int n) {
  unsigned v = 0, s;
  asm volatile("1:\n\tds_read_b32 %0, %2\n\ts_waitcnt lgkmcnt(0)\n\tv_readfirstlane_b32 %1, %0\n\ts_cmp_ge_u32 %1, %3\n\t"
               "s_cbranch_scc1 2f\n\ts_sleep %4\n\ts_branch 1b\n2:" : "+&v"(v), "=&s"(s) : "v"(off), "i"(n), "i"(SLEEP) : "memory", "scc");
}

constexpr int KCS = 4160;
constexpr int G_P = 4 * KCS, G_U = G_P + 16384;
constexpr int GRP = G_U + 4608;
constexpr int ZP = 768;
constexpr int L_SYNC = 0, L_TAB = 128, L_U = L_TAB + F_ALL * 1024, L_IMG = L_U + 12 * 1280, L_GRP = L_IMG + 10240, L_END = L_GRP + 3 * GRP;
enum { C_GOA, C_GOB, C_A1, C_A1D, C_B1, C_B1S, C_B1D, C_K, C_G = C_K + 3, C_A2D = C_G + 3, C_A3, C_B2D, C_PUBA, C_XI, C_Q, C_WL, C_TI, C_TAG = 31 };
#define SY(c) (L_SYNC + 4 * (c))

#define OPAQUE8(a) asm("" : "+v"((a)[0]), "+v"((a)[1]), "+v"((a)[2]), "+v"((a)[3]), "+v"((a)[4]), "+v"((a)[5]), "+v"((a)[6]), "+v"((a)[7]))
template <typename F> DI void dft64(const u4 (&ar)[4], const u4 (&ai)[4], const u4 (&b)[4], F f) {
  f4 er[4], ei[4];
  const f4 z = {0.f, 0.f, 0.f, 0.f};
#pragma unroll
  for (int c = 0; c < 4; ++c) { const int cc = (c >> 1) | ((c & 1) << 1); er[cc] = mfma16(ar[cc], b[cc], z); ei[cc] = mfma16(ai[cc], b[cc], z); }
#pragma unroll
  for (int rr = 0; rr < 4; ++rr) {
    const int r = (rr >> 1) | ((rr & 1) << 1);
    float t[8] = {er[0][r] + er[2][r], ei[0][r] + ei[2][r], er[0][r] - er[2][r], ei[0][r] - ei[2][r],
                  er[1][r] + er[3][r], ei[1][r] + ei[3][r], er[1][r] - er[3][r], ei[1][r] - ei[3][r]};
    OPAQUE8(t);
    float x[8] = {t[0] + t[4], t[1] + t[5], t[2] + t[7], t[3] - t[6], t[0] - t[4], t[1] - t[5], t[2] - t[7], t[3] + t[6]};
    OPAQUE8(x);
    f(r, x);
  }
}

template <typename F, int... I> DI void static_for_(F f, std::integer_sequence<int, I...>) { (f(std::integral_constant<int, I>{}), ...); }
template <int N, typename F> DI void static_for(F f) { static_for_(f, std::make_integer_sequence<int, N>{}); }
template <bool INV> DI void twiddle(const f16v& vr, const f16v& vi, const f4 (&tc)[3], const f4 (&tn)[3], u4& br, u4& bi, u4& bm) {
  unsigned pr[6], pi[6];
  f2 re[6], im[6];
  static_for<6>([&](auto jc) {
    constexpr int J = decltype(jc)::value;
    re[J] = half<J>(vi) * half<J & 1>(tn[J >> 1]); im[J] = half<J>(vr) * half<J & 1>(tn[J >> 1]);
  });
  SCHED();
  static_for<6>([&](auto jc) {
    constexpr int J = decltype(jc)::value;
    re[J] = __builtin_elementwise_fma(half<J>(vr), half<J & 1>(tc[J >> 1]), INV ? -re[J] : re[J]);
    im[J] = __builtin_elementwise_fma(half<J>(vi), half<J & 1>(tc[J >> 1]), INV ? im[J] : -im[J]);
  });
  SCHED();
#pragma unroll
  for (int j = 0; j < 6; ++j) { pr[j] = pk(re[j][0], re[j][1]); pi[j] = pk(im[j][0], im[j][1]); }
  br = (u4){pr[0], pr[1], pr[2], pr[3]}; bi = (u4){pi[0], pi[1], pi[2], pi[3]}; bm = (u4){pr[4], pr[5], pi[4], pi[5]};
}

struct ConvList { int n, a[36], b[36]; };
constexpr ConvList conv_list(int mask) {
  ConvList c{};
  for (int s = 0; s < 8; ++s) {
    for (int e = 0; e <= s; ++e) if (s + e < 8 && ((mask >> (s + e)) & 1)) { c.a[c.n] = s; c.b[c.n] = e; ++c.n; }
    for (int e = 0; e < s; ++e) if (s + e < 8 && ((mask >> (s + e)) & 1)) { c.a[c.n] = e; c.b[c.n] = s; ++c.n; }
  }
  return c;
}
constexpr int conv_rd(int n) {
  constexpr int RB[5] = {0, 4, 10, 14, 17};
  for (int f = 0; f < 5; ++f) if (n == RB[f]) return 3 * (3 + f);
  constexpr int RF[8] = {21, 24, 27, 32, 36, 39, 42, 45};
  for (int f = 0; f < 8; ++f) if (n >= RF[f] && n < RF[f] + 3) return 3 * (8 + f) + n - RF[f];
  return -1;
}
constexpr int conv_wr(int n) {
  constexpr int WF[7] = {49, 51, 53, 57, 63, 68, 71};
  for (int j = 0; j < 7; ++j) if (n == WF[j]) return j;
  return -1;
}
template <int MASK> DI void conv_part(char* base, int ao, int bo, int wo, int cg, const u4 (&a0)[8]) {
  constexpr int NB = (MASK & 0x80) ? 8 : 6;
  constexpr ConvList L = conv_list(MASK);
  constexpr bool FULL = MASK == 0xff;
  constexpr int PRO = FULL ? 3 : 4;
  f4 acc[8];
#pragma unroll
  for (int j = 0; j < 8; ++j) acc[j] = (f4){0.f, 0.f, 0.f, 0.f};
  u4 b[2 * NB]; u2 al[NB], ah[NB];
  auto rd = [&](int f, int part) {
    const int rr = f / NB, e = f % NB;
    if (part == 0) b[f] = *(const u4*)(base + bo + 512 * (7 - e) + 64 * rr);
    if (part == 1) al[e] = trread(base + ao + 512 * e + 64);
    if (part == 2) ah[e] = trread(base + ao + 512 * e + 64 + 8);
  };
  auto wr = [&](int j) { if ((MASK >> j) & 1) *(f4*)(base + wo + (j < 4 ? G_P + 4096 * j : 4096 * (j - 4))) = acc[j]; };
#pragma unroll
  for (int f = 0; f < PRO; ++f) { rd(f, 0); SCHED(); }
  constexpr int R1 = 9;
  constexpr int LASTRD = FULL ? 47 : R1 + (3 * NB - 1) / 2;
  unsigned seen = 0;
  static_for<2 * L.n>([&](auto nc) {
    constexpr int n = decltype(nc)::value, o = n < L.n ? 0 : NB, fa = L.a[n % L.n], fb = L.b[n % L.n];
    acc[fa + fb] = mfma16(n < L.n ? a0[fa] : cat(al[fa], ah[fa]), b[o + fb], acc[fa + fb]);
    SCHED();
    if constexpr (FULL) {
      constexpr int f = conv_rd(n), j = conv_wr(n);
      if constexpr (f >= 0) rd(f / 3, f % 3);
      if constexpr (n == 58) seen = *(volatile LDS unsigned*)(size_t)cg;
      if constexpr (j == 4) { if (__builtin_amdgcn_readfirstlane(seen) < 8) await<0>(cg, 8); }
      if constexpr (j >= 0) wr(j);
    } else {
      if constexpr (n < NB - PRO) rd(PRO + n, 0);
      constexpr int k = 2 * (n - R1);
      if constexpr (k >= 0 && k < 3 * NB) { rd(NB + k / 3, k % 3); rd(NB + (k + 1) / 3, (k + 1) % 3); }
    }
    if constexpr (n == LASTRD + 1) arrive(cg);
    SCHED();
  });
  if (FULL) wr(7);
  else {
    await<0>(cg, 8);
#pragma unroll
    for (int j = 0; j < 8; ++j) wr(j);
  }
}

template <bool STREAM> DI void glds16(const void* src, int dst) {
  unsigned keep;
  if (STREAM) asm volatile("s_mov_b32 %0, m0\n\ts_mov_b32 m0, %2\n\ts_nop 0\n\tglobal_load_lds_dwordx4 %1, off nt\n\ts_mov_b32 m0, %0"
                           : "=&s"(keep) : "v"(src), "s"(dst) : "memory");
  else asm volatile("s_mov_b32 %0, m0\n\ts_mov_b32 m0, %2\n\ts_nop 0\n\tglobal_load_lds_dwordx4 %1, off\n\ts_mov_b32 m0, %0"
                    : "=&s"(keep) : "v"(src), "s"(dst) : "memory");
}
DI unsigned now() { return (unsigned)__builtin_amdgcn_s_memrealtime(); }
DI void pace(unsigned t0, int x, int p) {
  const unsigned t1 = now();
  while ((int)(now() - t0) < x || (int)(now() - t1) < p) __builtin_amdgcn_s_sleep(1);
}
template <bool WIDE> DI void gather(rsrc rf, int off, unsigned tag) {
  SPIN_LOOP {
    CFENCE();
    bool ok;
    if (WIDE) { const u4 f = ld128<SC1>(rf, off, 0); ok = f[0] == tag && f[1] == tag && f[2] == tag && f[3] == tag; }
    else ok = ld32<SC1>(rf, off, 0) == tag;
    if (__all(ok)) break;
    __builtin_amdgcn_s_sleep(2);
  }
}

DI int fresh_lane() {
  unsigned z = 0;
  PIN(z);
  return __builtin_amdgcn_mbcnt_hi(~0u, __builtin_amdgcn_mbcnt_lo(~0u, z));
}
#define LANES() const int l = fresh_lane(), tid = 64 * w + l, i = l & 15, g = l >> 4, n32 = l & 31, h = l >> 5; char* tab = smem + L_TAB + 16 * l; \
  (void)tid; (void)i; (void)g; (void)n32; (void)h; (void)tab

extern "C" __global__ __launch_bounds__(1024, 4) void gfh3(const uint16_t* __restrict__ x, const uint16_t* __restrict__ wr, const uint16_t* __restrict__ wi,
                                                        const uint16_t* __restrict__ qin, const uint16_t* __restrict__ kin, const uint16_t* __restrict__ vin,
                                                        const uint16_t* __restrict__ shk, const uint16_t* __restrict__ ssk, const uint16_t* __restrict__ ssd,
                                                        uint16_t* __restrict__ out, char* ws) {
  __shared__ __attribute__((aligned(16))) char smem[L_END];
  const int l = threadIdx.x & 63;
  const int wv0 = (__builtin_amdgcn_readfirstlane(threadIdx.x >> 6) + 8) & 15;
  const int w = wv0 >= 12 ? wv0 ^ 1 : wv0;
  const int tid = 64 * w + l;
  const int xcd = blockIdx.x & 7, idx = blockIdx.x >> 3;
  const int q = idx & 3, s2 = xcd * 8 + (idx >> 2);
  const int rb = 8 * xcd + (idx >> 2), cgp = idx & 3;
  const int i = l & 15, g = l >> 4, n32 = l & 31, h = l >> 5;
  char* tab = smem + L_TAB + 16 * l;
#define FRAG(f) (*(u4*)(tab + 1024 * (f)))
#define FRAGF(f) (*(f4*)(tab + 1024 * (f)))
  const unsigned tstart = now();
  unsigned sink = 0;
  if (w == 8) {
    unsigned long long pc; int rel, len;
    asm volatile("s_getpc_b64 %0\n\ts_mov_b32 %1, gfh3@rel32@lo+4\n\ts_mov_b32 %2, gfh3_code_end-gfh3" : "=s"(pc), "=s"(rel), "=s"(len));
    const char* p = (const char*)(pc + (long long)rel) + min(128 * (8 * l + (idx & 7)), len);
    asm volatile("global_load_dword %0, %1, off" : "+v"(sink) : "v"(p) : "memory");
  }
  asm volatile("" :: "s"(x), "s"(wr), "s"(wi), "s"(qin), "s"(kin), "s"(vin), "s"(shk), "s"(ssk), "s"(ssd), "s"(out), "s"(ws));
  const rsrc rf = mkrs(ws + WS_FLAG, FL_EPOCH + 4);
  const int gi = w < 8 ? 2 : (w - 8) >> 2;
  bool conv = false;

  if (__builtin_expect((unsigned)(w - 8) < 5u, 1)) {
    const int t = tid - 512, c2 = t % 80, tc = t / 80;
    unsigned kr[11], vv[8], epoch;
    u4 wk;
    {
      const rsrc rk = mkrs(kin, 2048 * 1280), rv = mkrs(vin, 2048 * 1280), rs = mkrs(shk, 640 * 8);
      const int vo = (32 * rb + 8 * tc) * 1280 + 320 * cgp + 4 * c2;
      const int vh = (rb | tc) ? vo - 3 * 1280 : 2048 * 1280;
      epoch = ld32<SC1>(rf, w == 8 ? FL_EPOCH : FL_EPOCH + 4, 0);
      SCHED();
#pragma unroll
      for (int j = 0; j < 8; ++j) { kr[3 + j] = ld32<NT>(rk, vo, j * 1280); vv[j] = ld32<NT>(rv, vo, j * 1280); }
      wk = ld128(rs, 1280 * cgp + 16 * c2, 0);
#pragma unroll
      for (int j = 0; j < 3; ++j) kr[j] = ld32<NT>(rk, vh + j * 1280, 0);
      SCHED();
    }
    const rsrc ro = mkrs(ws + WS_KV + 192 * cgp * CP + 4032 - 64 * rb, 191 * CP + 64);
    int so = (t >> 2) * CP + 16 * (t & 3), ri[3];
#pragma unroll
    for (int j = 0; j < 3; ++j) {
      const int r = min((t >> 2) + 80 * j, 191), ch = r - (r + 3) / 6;
      ri[j] = L_IMG + (ch >> 1) * 128 + 16 * ((4 * (ch & 1) + (t & 3) + (ch >> 1)) & 7);
      PIN(ri[j]);
    }
    int wi0 = L_IMG + c2 * 128 + 16 * ((3 - tc + c2) & 7);
    PIN(so); PIN(wi0);
    SCHED();
    asm volatile("" : "+v"(sink), "+v"(epoch));
    if (w == 8) {
      *(unsigned*)(smem + SY(C_TAG)) = epoch * 0x9E3779B1u + 0x7F4A7C15u;
    }
    LBAR();
    __builtin_amdgcn_s_setprio(3);
    {
      const f2 w0 = {blo(wk[0]), blo(wk[2])}, w1 = {bhi(wk[0]), bhi(wk[2])}, w2 = {blo(wk[1]), blo(wk[3])}, w3 = {bhi(wk[1]), bhi(wk[3])};
      f2 kf[11], o[8];
#pragma unroll
      for (int j = 0; j < 11; ++j) kf[j] = unp(kr[j]);
#pragma unroll
      for (int j = 0; j < 8; ++j) o[j] = (kf[j] * w3 + kf[j + 1] * w2 + kf[j + 2] * w1 + kf[j + 3] * w0) * unp(vv[j]);
#pragma unroll
      for (int e = 0; e < 2; ++e) {
        const u4 r = {pk(o[7][e], o[6][e]), pk(o[5][e], o[4][e]), pk(o[3][e], o[2][e]), pk(o[1][e], o[0][e])};
        *(u4*)(smem + (wi0 ^ (64 * e))) = r & ~1u;
      }
    }
    arrive(SY(C_B1));
    await<0>(SY(C_B1), 5);
#pragma unroll
    for (int j = 0; j < 3; ++j) st128<SC1>(*(const u4*)(smem + ri[j]), ro, so + j * 80 * CP);
    arrive(SY(C_B1S));
    conv = true;
  } else if (__builtin_expect(w < 5, 1)) {
    u4 xv[8];
    const rsrc rx = mkrs(x + s2 * 640 + 128 * w, 0x7fffffff);
    const int vx = g * (512 * 1280) + 16 * i;
#pragma unroll
    for (int j = 0; j < 2; ++j) xv[j] = ld128(rx, vx, (j + 2 * q) * 64 * 1280);
    SCHED();
    u4 m1;
    {
      const int wv = i >> 1;
      const float kf = (wv < 4 ? (float)(4 * q + wv + 1) : 0.f) * (1.f / 2048.f);
      const float ph = 0.25f * (float)(i & 1);
#pragma unroll
      for (int jj = 0; jj < 4; ++jj) {
        const float sb = (float)(512 * g + s2 + 128 * ((jj + q) & 3));
        m1[jj] = pk(fcos(fmaf(kf, sb, ph)), fcos(fmaf(kf, sb + 64.f, ph)));
      }
    }
    const int ks1 = w < 4 ? 4 * q + w + 1 : 0;
    const rsrc rt = mkrs(ws + WS_T + s2 * TP * 4, 32 * SLOT * 4);
    const int cs = h ? (16 - ks1) * SLOT + (ks1 ? 0 : TP) : (ks1 + 16) * SLOT;
    int so = 4 * (cs + (h ? (32 - n32) & 31 : n32));
    int sx = 4 * (cs + 320 + n32 + (n32 < 16 ? 0 : TP));
    int upo = L_U + 2 * w * 1280 + 40 * (8 * h + ((l >> 2) & 3)) + 8 * min(4 * ((l >> 4) & 1) + (l & 3), 4);
    int wro = L_U + 4 * g * 1280 + 256 * w + 16 * i;
    PIN(m1); PIN(so); PIN(sx); PIN(upo); PIN(wro);
    BAR();
#pragma unroll
    for (int j = 2; j < 8; ++j) xv[j] = ld128(rx, vx, ((j + 2 * q) & 7) * 64 * 1280);
    arrive(SY(C_XI));
    u4 C0 = FRAG(F_C0), S0 = FRAG(F_S0);
    f4 tc[3], tn[3];
#pragma unroll
    for (int j = 0; j < 3; ++j) { tc[j] = FRAGF(F_TC + j); PIN(tc[j]); }
    const unsigned sg = (n32 & 1) ? SGN2 : 0u;
    u4 N0 = S0 ^ SGN2, C1 = C0 ^ sg, S1 = S0 ^ sg, N1 = N0 ^ sg;
    PIN(C0); PIN(S0); PIN(N0); PIN(C1); PIN(S1); PIN(N1);
    SCHED();
    __builtin_amdgcn_s_setprio(3);
    {
      f4 acc[8];
      u4 b[8];
#pragma unroll
      for (int jj = 0; jj < 4; ++jj) {
#pragma unroll
        for (int t = 0; t < 8; ++t) {
          b[t][jj] = __builtin_amdgcn_perm(xv[2 * jj + 1][t >> 1], xv[2 * jj][t >> 1], (t & 1) ? 0x07060302u : 0x05040100u);
          const f4 z = {0.f, 0.f, 0.f, 0.f};
          if (jj == 3) acc[t] = mfma16(m1, b[t], z);
        }
        SCHED();
      }
      if (g < 3) {
#pragma unroll
        for (int r = 0; r < 4; ++r) {
          u4 o = {pk(acc[0][r], acc[1][r]), pk(acc[2][r], acc[3][r]), pk(acc[4][r], acc[5][r]), pk(acc[6][r], acc[7][r])};
          *(u4*)(smem + wro + r * 1280) = o;
        }
      }
    }
    arrive(SY(C_A1));
    if (w < 4 || q == 0) {
      const u4 PC = FRAG(F_PC), PS = FRAG(F_PS), MR = FRAG(F_MR);
#pragma unroll
      for (int j = 0; j < 3; ++j) tn[j] = FRAGF(F_TS + j);
      await<0>(SY(C_A1), 5);
      const char* up = smem + upo;
      u4 are[2], aim[2];
#pragma unroll
      for (int kk = 0; kk < 2; ++kk) {
        are[kk] = cat(trread(up + 640 * kk), trread(up + 640 * kk + 160));
        aim[kk] = cat(trread(up + 1280 + 640 * kk), trread(up + 1280 + 640 * kk + 160));
      }
      f16v vr = {}, vi = {};
      vr = mfma32(are[0], C0, vr); vi = mfma32(are[0], N0, vi);
      vr = mfma32(aim[0], S0, vr); vi = mfma32(aim[0], C0, vi);
      vr = mfma32(are[1], C1, vr); vi = mfma32(are[1], N1, vi);
      vr = mfma32(aim[1], S1, vr); vi = mfma32(aim[1], C1, vi);
      const u4 PN = PS ^ SGN2, MI = {MR[2] ^ SGN2, MR[3] ^ SGN2, MR[0], MR[1]};
      u4 br, bi, bm;
      twiddle<false>(vr, vi, tc, tn, br, bi, bm);
      f16v xr = {}, xi = {};
      xr = mfma32(PC, br, xr); xi = mfma32(PN, br, xi);
      xr = mfma32(PS, bi, xr); xi = mfma32(PC, bi, xi);
      xr = mfma32(MR, bm, xr); xi = mfma32(MI, bm, xi);
      const unsigned cj = h ? 0x80000000u : 0u;
      unsigned o[10];
#pragma unroll
      for (int r = 0; r < 10; ++r) o[r] = (pk(xr[r], xi[r]) ^ cj) & ~1u;
      const auto sw = __builtin_amdgcn_permlane32_swap(o[9], o[0], false, false);
#pragma unroll
      for (int r = 1; r < 11; ++r) st32<SC1>(l == 32 ? (r < 10 ? o[r - 1] : sw[0] ^ 0x80000000u) : o[r % 10], rt, so + 128 * (r % 10), 0);
      st32<SC1>(h ? o[9] : sw[1] ^ 0x80000000u, rt, sx, 0);
    }
    if (w == 4) {
      LANES();
      const char* qp[10];
#pragma unroll
      for (int j = 0; j < 10; ++j) {
        const int c = l + 64 * j;
        qp[j] = (const char*)(qin + (32 * rb + c / 20) * 640 + 160 * cgp + 8 * (c % 20));
        PIN(qp[j]);
      }
      drain();
      arrive(SY(C_A1D));
      const unsigned tag = *(const unsigned*)(smem + SY(C_TAG));
      await<1>(SY(C_A1D), 5);
      if (l < 8) st32<SC1>(tag, rf, FL_A1 + 1024 * l + 4 * blockIdx.x, 0);
      arrive(SY(C_PUBA));
      await<1>(SY(C_B1D), 5);
      pace(tstart, XA, 35);
      gather<true>(rf, FL_A1 + 1024 * xcd + 16 * l, tag);
      arrive(SY(C_GOA));
      if (idx < 28) await<0>(SY(C_TI), 3);
#pragma unroll
      for (int j = 0; j < 10; ++j) glds16<true>(qp[j], L_IMG + 1024 * j);
      drain();
      arrive(SY(C_Q));
      if (blockIdx.x == 255 && l == 0) st32<SC1>(tag, rf, FL_EPOCH, 0);
    }
    conv = w < 4;
  } else if (w < 8) {
    const bool act = idx < 28;
    const int slot = 4 * xcd + idx / 7, tile = 3 * (idx % 7) + (w - 5);
    if (w < 7) {
      const int ro = (n32 & 3) + 4 * (n32 >> 3);
      const float a20 = w == 5 ? (float)((n32 & 4) ? 19 - ro : ro) * 0.05f : (float)n32 * -0.05f;
      u4 c, s, m;
#pragma unroll
      for (int jj = 0; jj < 4; ++jj) {
        const float a = a20 * (float)(8 * (jj >> 1) + 4 * h + 2 * (jj & 1)), b = a + a20;
        c[jj] = pk(fcos(a), fcos(b)); s[jj] = pk(fsin(a), fsin(b));
      }
#pragma unroll
      for (int jj = 0; jj < 2; ++jj) {
        const float a = a20 * (float)(16 + 2 * jj), b = a + a20;
        m[jj] = h ? 0u : pk(fcos(a), fcos(b)); m[2 + jj] = h ? 0u : pk(fsin(a), fsin(b));
      }
      char* tp = tab + (w - 5) * 3072;
      *(u4*)(tp + 1024 * F_PC) = c; *(u4*)(tp + 1024 * F_PS) = s; *(u4*)(tp + 1024 * F_MR) = m;
    } else {
      const float a64 = (float)i * (1.f / 64.f);
#pragma unroll
      for (int c = 0; c < 4; ++c) {
        u4 v;
#pragma unroll
        for (int u = 0; u < 4; ++u) { const float a = a64 * (float)(16 * u + c + 4 * g); v[u] = pk(fcos(a), fsin(a)); }
        FRAG(F_AR + c) = v;
      }
    }
    LBAR();
    u4 ar[4], ai[4];
#pragma unroll
    for (int c = 0; c < 4; ++c) {
      ar[c] = FRAG(F_AR + c);
#pragma unroll
      for (int u = 0; u < 4; ++u) ai[c][u] = __builtin_amdgcn_alignbit(ar[c][u], ar[c][u], 16) ^ 0x8000u;
      PIN(ar[c]); PIN(ai[c]);
    }
    const rsrc rt = mkrs(ws + WS_T, 64 * TP * 4);
    int vo = 4 * (4 * g * TP + slot * SLOT + 16 * tile + i);
    const int w0 = (slot - 16) * 321 + 48 * (idx % 7);
    const char* gp[5];
#pragma unroll
    for (int j = 0; j < 5; ++j) {
      const int n = min(3 * j + (w - 5), 13), c = l + 64 * (n % 7);
      int o = 2 * ((w0 & ~7) + (c / 7) * NCOL) + 16 * (c % 7);
      o += (o < 0) ? 128 * NCOL : 0;
      gp[j] = (const char*)(n < 7 ? wr : wi) + min(o, 128 * NCOL - 16);
      PIN(gp[j]);
    }
    PIN(vo);
    if (w < 7) {
      int o = 2 * (1284 * xcd - 16 * 321 - 16 + (2 * idx + h) * NCOL) + 128 * n32;
      o += (o < 0) ? 128 * NCOL : 0;
      await<1>(SY(C_B1S), 5);
      const unsigned t0 = ld32(mkrs(w == 5 ? wr : wi, 128 * NCOL), n32 < 22 ? o : 128 * NCOL, 0);
      asm volatile("" :: "v"(t0));
    }
    await<1>(SY(C_PUBA), 1);
    __builtin_amdgcn_s_setprio(3);
    if (act) {
#pragma unroll
      for (int j = 0; j < 5; ++j)
        if (j < 4 || w < 7) glds16<false>(gp[j], L_U + 1024 * (3 * j + (w - 5)));
    }
    drain();
    arrive(SY(C_WL));
    await<0>(SY(C_WL), 3);
    f2 wa[4][2], wb[4][2];
    const int kd = 16 * tile + i;
    const float wsc2 = (kd == 0 || kd == 320) ? 0.5f : 1.f;
#pragma unroll
    for (int u = 0; u < 4; ++u) {
#pragma unroll
      for (int r = 0; r < 4; ++r) {
        const char* p = smem + L_U + 448 * g + 2 * (i + 16 * (w - 5) + (w0 & 7)) + 1792 * u + 112 * r;
        wa[u][r >> 1][r & 1] = blo(*(const uint16_t*)p); wb[u][r >> 1][r & 1] = blo(*(const uint16_t*)(p + 7168));
      }
      PIN(wa[u][0]); PIN(wb[u][0]); PIN(wa[u][1]); PIN(wb[u][1]);
    }
    if (tile == 0 || tile == 20) {
#pragma unroll
      for (int e = 0; e < 16; ++e) {
        float ta = wa[e >> 2][(e >> 1) & 1][e & 1] * wsc2, tb = wb[e >> 2][(e >> 1) & 1][e & 1] * wsc2;
        asm("" : "+v"(ta), "+v"(tb));
        wa[e >> 2][(e >> 1) & 1][e & 1] = ta; wb[e >> 2][(e >> 1) & 1][e & 1] = tb;
      }
    }
    await<0>(SY(C_GOA), 1);
    if (act) {
      u4 b[4];
#pragma unroll
      for (int c = 0; c < 4; ++c) {
        const int cc = (c >> 1) | ((c & 1) << 1);
#pragma unroll
        for (int u = 0; u < 4; ++u) b[cc][u] = ld32<SC1>(rt, vo, (16 * u + cc) * TP * 4);
      }
      SCHED();
      arrive(SY(C_TI));
      SCHED();
      u4 b2[4];
      dft64(ar, ai, b, [&](int r, float (&x)[8]) {
        float y[8];
#pragma unroll
        for (int u = 0; u < 4; ++u) {
          const float a = wa[u][r >> 1][r & 1], c = wb[u][r >> 1][r & 1];
          y[2 * u] = fmaf(x[2 * u], a, -(x[2 * u + 1] * c)); y[2 * u + 1] = fmaf(-x[2 * u], c, -(x[2 * u + 1] * a));
        }
        OPAQUE8(y);
#pragma unroll
        for (int u = 0; u < 4; ++u) b2[r][u] = pk(y[2 * u], y[2 * u + 1]);
      });
      dft64(ar, ai, b2, [&](int r, float (&x)[8]) {
#pragma unroll
        for (int u = 0; u < 4; ++u) st32<SC1>(pk(x[2 * u], x[2 * u + 1]) | 1u, rt, vo, (16 * u + r) * TP * 4);
      });
    } else {
      __builtin_amdgcn_s_sleep(40);
    }
    __builtin_amdgcn_s_setprio(0);
    arrive(SY(C_A2D));
  } else {
    if (w == 15) {
      if (l < 31) *(unsigned*)(smem + L_SYNC + 4 * l) = 0u;
      unsigned zero = 0;
      PIN(zero);
      if (l == 0 && blockIdx.x < 72)
        __hip_atomic_fetch_or((GLB unsigned*)(ws + WS_FLAG + 128 * blockIdx.x), zero, __ATOMIC_RELAXED, __HIP_MEMORY_SCOPE_AGENT);
    }
    if (w == 13) {
      const float kf = (float)n32, a32 = kf * (1.f / 32.f), a640 = kf * (1.f / 640.f);
      u4 c, s;
#pragma unroll
      for (int jj = 0; jj < 4; ++jj) {
        const float a = a32 * (float)(8 * h + 2 * jj), b = a + a32;
        c[jj] = pk(fcos(a), fcos(b)); s[jj] = pk(fsin(a), fsin(b));
      }
      FRAG(F_C0) = c; FRAG(F_S0) = s;
#pragma unroll
      for (int rg = 0; rg < 3; ++rg) {
        f4 tc, tn;
#pragma unroll
        for (int jj = 0; jj < 4; ++jj) { const float a = a640 * (float)(jj + 8 * rg + 4 * h); tc[jj] = fcos(a); tn[jj] = fsin(a); }
        FRAGF(F_TC + rg) = tc; FRAGF(F_TS + rg) = tn;
      }
    }
    if (w == 14) {
      u4 m3[2];
      const float scale = 2.f / (2048.f * 640.f);
      const float sf = (float)(64 * (8 * q + (i >> 1)) + s2) * (1.f / 2048.f);
      const float ph = -0.25f * (float)(i & 1);
#pragma unroll
      for (int j = 0; j < 8; ++j) {
        const int jr = (j + 2 * q) & 7;
        const float a = fmaf(sf, (float)(16 * (jr >> 2) + (jr & 3) - 16 + 4 * g), ph);
        m3[j >> 2][j & 3] = pk(fcos(a) * scale, fsin(a) * scale);
      }
      FRAG(F_M3) = m3[0]; FRAG(F_M3 + 1) = m3[1];
    }
    LBAR();
    await<0>(SY(C_XI), 5);
    const int gk = w - 13, ck = 80 * xcd + 5 * (idx >> 1) + (gk == 2 ? 2 : gk + 3 * (idx & 1));
    const rsrc rk = mkrs(ssk + ck * 2048, 4096);
    const unsigned dk = __builtin_amdgcn_raw_buffer_load_b16(mkrs(ssd, 1280), 2 * ck, 0, 0);
    u4 kc[4]; u2 kp[4];
#pragma unroll
    for (int j = 0; j < 4; ++j) kc[j] = ld128<NT>(rk, 16 * l + 1024 * j, 0);
    SCHED();
#pragma unroll
    for (int j = 0; j < 4; ++j)
#pragma unroll
      for (int e = 0; e < 2; ++e) {
        const unsigned up = __builtin_amdgcn_update_dpp(0u, kc[j][2 + e], 0x138, 0xf, 0xf, false);
        kp[j][e] = l ? up : (j ? __builtin_amdgcn_readlane(kc[j - 1][2 + e], 63) : 0u);
      }
    if (l == 0) kc[0][0] = pk(blo(kc[0][0]) + blo(dk), bhi(kc[0][0]));
    char* base = smem + L_GRP + gk * GRP;
    const u4 z4 = {0u, 0u, 0u, 0u};
    if (l < 40) *(u4*)(base + (l < 8 ? (l >> 1) * KCS + 16 * (l & 1) : G_U + 4096 + 16 * (l - 8))) = z4;
#pragma unroll
    for (int j = 0; j < 4; ++j) {
      const unsigned o0 = __builtin_amdgcn_alignbit(kp[j][1], kp[j][0], 16), o1 = __builtin_amdgcn_alignbit(kc[j][0], kp[j][1], 16),
                     o2 = __builtin_amdgcn_alignbit(kc[j][1], kc[j][0], 16), o3 = __builtin_amdgcn_alignbit(kc[j][2], kc[j][1], 16),
                     o4 = __builtin_amdgcn_alignbit(kc[j][3], kc[j][2], 16);
      char* kp0 = base + 32 + 16 * l + 1024 * j;
      const u4 c1 = {o1, o2, o3, o4}, c2 = {kp[j][1], kc[j][0], kc[j][1], kc[j][2]}, c3 = {o0, o1, o2, o3};
      *(u4*)(kp0) = kc[j]; *(u4*)(kp0 + KCS) = c1; *(u4*)(kp0 + 2 * KCS) = c2; *(u4*)(kp0 + 3 * KCS) = c3;
    }
    arrive(SY(C_K + gk));
    conv = true;
  }

  if (conv) {
    LANES();
    const int wl = w & 3, t = tid & 255;
    char* base = smem + L_GRP + gi * GRP;
    char* ul = base + G_U;
    const rsrc ru = mkrs(ws + WS_KV + (96 * xcd + 3 * idx + (gi == 2 ? 2 - 2 * (idx & 1) : gi + (idx & 1))) * CP, 4096);
    int ao = (3 - (i >> 2)) * KCS + 16 * g + 8 * (i & 3) + 8 + 128 * wl;
    int bo = G_U + 4064 - 3584 - 32 * i + 16 * g + 128 * wl;
    int wo = wl * 1024 + 16 * (4 * i + ((g + (i >> 1)) & 3));
    const int c = (t >> 1) & 15, r0 = (wl < 2 ? G_P : -16384) + 4096 * (t >> 5) + 64 * c;
    int p0 = r0 + 16 * ((2 * (t & 1) + (c >> 1)) & 3), p1 = r0 + 16 * ((2 * (t & 1) + 1 + (c >> 1)) & 3);
    const int mask = gi < 2 ? 0xff : ((idx & 1) ? 0x3b : 0xc4);
    int yo = ((mask >> (t >> 5)) & 1) ? 4080 - 16 * t : 4096;
    PIN(ao); PIN(bo); PIN(wo); PIN(p0); PIN(p1); PIN(yo);
    if (w < 13) {
      drain();
      arrive(w < 4 ? SY(C_A1D) : SY(C_B1D));
    }
    if (w == 12) {
      const unsigned tag = *(const unsigned*)(smem + SY(C_TAG));
      await<0>(SY(C_B1D), 5);
      if (l == 0) st32<SC1>(tag, rf, FL_B1 + 256 * cgp + 4 * rb, 0);
      pace(tstart, XB, 35);
      gather<false>(rf, FL_B1 + 256 * (xcd >> 1) + 4 * l, tag);
      arrive(SY(C_GOB));
    }
    u4 a0[8];
    await<1>(SY(C_K + gi), 1);
#pragma unroll
    for (int e = 0; e < 8; ++e) { a0[e] = cat(trread(base + ao + 512 * e), trread(base + ao + 512 * e + 8)); PIN(a0[e]); }
    __builtin_amdgcn_s_setprio(3);
    await<1>(SY(C_GOB), 1);
    const u4 uv = ld128<SC1>(ru, 16 * t, 0);
    *(u4*)(ul + 16 * t) = uv;
    const int cg = SY(C_G + gi);
    arrive(cg);
    await<0>(cg, 4);
    if (gi == 2) __builtin_amdgcn_s_setprio(2);
    if (gi == 0) __builtin_amdgcn_s_setprio(1);
    if (gi == 1) __builtin_amdgcn_s_setprio(0);
    if (gi < 2) conv_part<0xff>(base, ao, bo, wo, cg, a0);
    else if (idx & 1) conv_part<0x3b>(base, ao, bo, wo, cg, a0);
    else conv_part<0xc4>(base, ao, bo, wo, cg, a0);
    arrive(cg);
    await<0>(cg, 12);
    f4 pa[4], pb[4];
#pragma unroll
    for (int ww = 0; ww < 4; ++ww) { pa[ww] = *(const f4*)(base + p0 + ww * 1024); pb[ww] = *(const f4*)(base + p1 + ww * 1024); }
    float sm[8], sn[8];
#pragma unroll
    for (int e = 0; e < 4; ++e) { sm[e] = pa[0][e] + pa[1][e]; sm[4 + e] = pb[0][e] + pb[1][e]; sn[e] = pa[2][e] + pa[3][e]; sn[4 + e] = pb[2][e] + pb[3][e]; }
    OPAQUE8(sm); OPAQUE8(sn);
#pragma unroll
    for (int e = 0; e < 8; ++e) sm[e] += sn[e];
    OPAQUE8(sm);
    const u4 o = {pk(sm[7], sm[6]), pk(sm[5], sm[4]), pk(sm[3], sm[2]), pk(sm[1], sm[0])};
    st128<SC1>(o | 1u, ru, yo);
    __builtin_amdgcn_s_setprio(0);
    arrive(SY(C_B2D));
  }

  if (w < 8) {
    LANES();
    u4 C0 = FRAG(F_C0), S0 = FRAG(F_S0), QC = FRAG(F_QC), QS = FRAG(F_QS), QM = FRAG(F_QM);
    f4 tc[3], tn[3];
#pragma unroll
    for (int j = 0; j < 3; ++j) { tc[j] = FRAGF(F_TC + j); tn[j] = FRAGF(F_TS + j); PIN(tc[j]); PIN(tn[j]); }
    u4 N0 = S0 ^ SGN2;
    PIN(C0); PIN(S0); PIN(N0); PIN(QC); PIN(QS); PIN(QM);
    int zo = L_U + 2 * w * ZP + 40 * (8 * h + ((l >> 2) & 3)) + 8 * min(4 * ((l >> 4) & 1) + (l & 3), 4);
    int zw = L_U + 2 * w * ZP + 72 * min(n32, 20) + 8 * h, zs = L_U + 2 * w * ZP + 72 * (l >> 2) + 16 * (l & 3);
    const rsrc ro = mkrs(out + (64 * (8 * q + w) + s2) * 1280, 1280);
    PIN(zo); PIN(zw); PIN(zs);
    asm volatile("" :: "s"(ro));
    if (w < 6) {
      u4 m3[2] = {FRAG(F_M3), FRAG(F_M3 + 1)};
      const rsrc rt = mkrs(ws + WS_T + s2 * TP * 4, TP * 4);
      int vo = 4 * (4 * g * SLOT + (w < 5 ? 64 * w + 4 * i : 320));
      int wo = L_U + 4 * g * ZP + 128 * w + 8 * i;
      PIN(m3[0]); PIN(m3[1]); PIN(vo); PIN(wo);
      await<2>(SY(C_A2D), 3);
      __builtin_amdgcn_s_sleep(D_X2);
      pace(tstart, X2A, 0);
      __builtin_amdgcn_s_setprio(3);
      const f4 z = {0.f, 0.f, 0.f, 0.f};
      bool bad = true;
      if (w < 5) {
        u4 tv[8];
        f4 acc[4];
        SPIN_LOOP {
          CFENCE();
          if (bad) {
#pragma unroll
            for (int j = 0; j < 8; ++j) { const int jr = (j + 2 * q) & 7; tv[j] = ld128<SC1>(rt, vo, 4 * ((jr & 3) + 16 * (jr >> 2)) * SLOT); }
          }
          unsigned m = 1u;
#pragma unroll
          for (int kk = 0; kk < 2; ++kk) {
#pragma unroll
            for (int t = 0; t < 4; ++t) {
              const u4 b = {tv[4 * kk][t], tv[4 * kk + 1][t], tv[4 * kk + 2][t], tv[4 * kk + 3][t]};
              acc[t] = mfma16(m3[kk], b, kk ? acc[t] : z);
              m = and4(m, b);
            }
            SCHED();
          }
          bad = m == 0u;
          if (!__any(bad)) break;
          __builtin_amdgcn_s_sleep(D_RETRY);
        }
#pragma unroll
        for (int r = 0; r < 4; ++r) {
          const u2 o = {pk(acc[0][r], acc[1][r]), pk(acc[2][r], acc[3][r])};
          *(u2*)(smem + wo + r * ZP) = o;
        }
      } else {
        u4 nv[2];
        SPIN_LOOP {
          CFENCE();
#pragma unroll
          for (int j = 0; j < 8; ++j) { const int jr = (j + 2 * q) & 7; nv[j >> 2][j & 3] = ld32<SC1>(rt, vo, 4 * ((jr & 3) + 16 * (jr >> 2)) * SLOT); }
          if (__all(and4(and4(1u, nv[0]), nv[1]) != 0u)) break;
          __builtin_amdgcn_s_sleep(D_RETRY);
        }
        const f4 acc = mfma16(m3[1], nv[1], mfma16(m3[0], nv[0], z));
        *(float*)(smem + wo) = acc[0]; *(float*)(smem + wo + 2 * ZP) = acc[2];
      }
      arrive(SY(C_A3));
    }
    await<0>(SY(C_A3), 6);
    {
      const char* zp = smem + zo;
      const u4 are = cat(trread(zp), trread(zp + 160)), aim = cat(trread(zp + ZP), trread(zp + ZP + 160));
      const float nyq = *(const float*)(smem + L_U + 2 * w * ZP + 640);
      f16v vr = {}, vi = {};
      vr = mfma32(are, C0, vr); vi = mfma32(are, S0, vi);
      vr = mfma32(aim, N0, vr); vi = mfma32(aim, C0, vi);
      f16v y;
#pragma unroll
      for (int r = 0; r < 16; ++r) y[r] = (r & 1) ? -nyq : nyq;
      u4 br, bi, bm;
      twiddle<true>(vr, vi, tc, tn, br, bi, bm);
      y = mfma32(br, QC, y); y = mfma32(bi, QS, y); y = mfma32(bm, QM, y);
#pragma unroll
      for (int t = 0; t < 4; ++t) {
        const u2 o = {pk(y[4 * t], y[4 * t + 1]), pk(y[4 * t + 2], y[4 * t + 3])};
        *(u2*)(smem + zw + 16 * t) = o;
      }
      CFENCE();
      const char* zq = smem + zs;
      const u4 o0 = cat(*(const u2*)zq, *(const u2*)(zq + 8)), o1 = cat(*(const u2*)(zq + 72 * 16), *(const u2*)(zq + 72 * 16 + 8));
      st128<SC1>(o0, ro, 16 * l);
      st128<SC1>(o1, ro, 16 * l + 1024);
    }
  } else if (w < 13) {
    LANES();
    const int t = tid - 512;
    constexpr int L_Y = L_GRP;
    u4 yv[2];
    const rsrc ry = mkrs(ws + WS_KV + 192 * cgp * CP + 4032 - 64 * rb, 192 * CP);
    int yi = ((t >> 2) + ((t >> 2) + 2 + ((0x3b >> xcd) & 1)) / 5) * CP + 16 * (t & 3);
    PIN(yi);
    await<2>(SY(C_B2D), 12);
    __builtin_amdgcn_s_sleep(D_X2);
    pace(tstart, X2B, 0);
    __builtin_amdgcn_s_setprio(3);
#pragma unroll
    for (int j = 0; j < 2; ++j) yv[j] = ld128<SC1>(ry, yi, j * 96 * CP);
    SCHED();
    const int c8 = t >> 4;
    await<0>(SY(C_Q), 1);
    f2 qf[2][4];
#pragma unroll
    for (int j = 0; j < 2; ++j) {
      const u4 qv = *(const u4*)(smem + L_IMG + (31 - i - 16 * j) * 320 + 16 * c8);
#pragma unroll
      for (int e = 0; e < 4; ++e) { qf[j][e] = unp(qv[e]); PIN(qf[j][e]); }
    }
    int wo = L_Y + (t >> 2) * 80 + 16 * (t & 3);
    int po = L_Y + (8 * c8 + (i >> 2)) * 80 + 8 * (i & 3), oo = (15 - (l >> 2)) * 2560 + 64 * (w - 8) + 16 * (l & 3);
    int xo = L_Y + 16384 + 2560 * (w - 8) + 80 * i + 16 * g, xi = L_Y + 16384 + 2560 * (w - 8) + 80 * (l >> 2) + 16 * (l & 3);
    const rsrc ro = mkrs(out + 32 * rb * 1280 + 640 + 160 * cgp, 32 * 2560);
    asm volatile("" :: "s"(ro));
    PIN(wo); PIN(po); PIN(oo); PIN(xo); PIN(xi);
    SCHED();
    SPIN_LOOP {
      const bool bad = and4(and4(1u, yv[0]), yv[1]) == 0u;
      if (!__any(bad)) break;
      __builtin_amdgcn_s_sleep(D_RETRY);
      CFENCE();
      if (bad) {
#pragma unroll
        for (int j = 0; j < 2; ++j) yv[j] = ld128<SC1>(ry, yi, j * 96 * CP);
      }
    }
#pragma unroll
    for (int j = 0; j < 2; ++j) *(u4*)(smem + wo + 80 * 80 * j) = yv[j];
    arrive(SY(C_B1));
    await<0>(SY(C_B1), 10);
    u4 o[2];
#pragma unroll
    for (int j = 0; j < 2; ++j) {
      const char* p = smem + po + 32 * j;
      const u4 y = cat(trread(p), trread(p + 4 * 80));
      float v[8];
#pragma unroll
      for (int e = 0; e < 4; ++e) { v[2 * e] = blo(y[e]) * qf[j][e][0]; v[2 * e + 1] = bhi(y[e]) * qf[j][e][1]; }
      OPAQUE8(v);
#pragma unroll
      for (int e = 0; e < 4; ++e) o[j][e] = pk(v[2 * e], v[2 * e + 1]);
      *(u4*)(smem + xo + 1280 * j) = o[j];
    }
    CFENCE();
#pragma unroll
    for (int j = 0; j < 2; ++j) st128<SC1>(*(const u4*)(smem + xi + 1280 * j), ro, oo + (1 - j) * 16 * 2560);
  }
}

extern "C" __device__ __attribute__((used, noinline)) void gfh3_code_end() {}

extern "C" void kernel_launch(void* const* d_in, const int* in_sizes, int n_in, void* d_out, int out_size, void* d_ws,
                              size_t ws_size, hipStream_t stream) {
  gfh3<<<256, 1024, 0, stream>>>((const uint16_t*)d_in[0], (const uint16_t*)d_in[1], (const uint16_t*)d_in[2], (const uint16_t*)d_in[3],
                                 (const uint16_t*)d_in[4], (const uint16_t*)d_in[5], (const uint16_t*)d_in[6], (const uint16_t*)d_in[7],
                                 (const uint16_t*)d_in[8], (uint16_t*)d_out, (char*)d_ws);
}
```

```cpp
#include <hip/hip_runtime.h>
#include <stdint.h>
#include <utility>

typedef __attribute__((ext_vector_type(2))) __bf16 bf2;
typedef __attribute__((ext_vector_type(8))) __bf16 bf8;
typedef __attribute__((ext_vector_type(2))) float f2;
typedef __attribute__((ext_vector_type(4))) float f4;
typedef __attribute__((ext_vector_type(16))) float f16v;
typedef __attribute__((ext_vector_type(2))) unsigned u2;
typedef __attribute__((ext_vector_type(4))) unsigned u4;
typedef __attribute__((ext_vector_type(4))) short s4;
typedef __amdgpu_buffer_rsrc_t rsrc;
#define DI __device__ __forceinline__
#define LDS __attribute__((address_space(3)))
#define GLB __attribute__((address_space(1)))
#define PIN(x) asm volatile("" : "+v"(x))
#define SCHED() __builtin_amdgcn_sched_barrier(0)
#define BAR() asm volatile("s_barrier" ::: "memory")
#define LBAR() asm volatile("s_waitcnt lgkmcnt(0)\n\ts_barrier" ::: "memory")
#define CFENCE() asm volatile("" ::: "memory")
#define SPIN_LOOP _Pragma("unroll 1") for (unsigned spin = 0; spin < SPIN_MAX; ++spin)

constexpr int XA = 495, XB = 422, X2A = 773, X2B = 829;
constexpr int D_X2 = 6, D_RETRY = 4;
constexpr int NT = 2;
constexpr int SC1 = 16;
constexpr int NCOL = 32 * 321;
constexpr int SLOT = 352;
constexpr int TP = 32 * SLOT + 32;
constexpr int CP = 4096 + 128;
constexpr unsigned SGN2 = 0x80008000u;
constexpr unsigned SPIN_MAX = 1u << 18;

constexpr int WS_T = 0, WS_KV = 64 * TP * 4, WS_FLAG = WS_KV + 768 * CP, FL_A1 = 0, FL_B1 = 8 * 1024, FL_EPOCH = 9 * 1024;

enum { F_C0, F_S0, F_PC, F_PS, F_MR, F_QC, F_QS, F_QM, F_TC, F_TS = F_TC + 3, F_AR = F_TS + 3, F_M3 = F_AR + 4, F_ALL = F_M3 + 2 };

DI unsigned pk(float a, float b) { f2 v = {a, b}; return __builtin_bit_cast(unsigned, __builtin_convertvector(v, bf2)); }
DI float blo(unsigned v) { return __uint_as_float(v << 16); }
DI float bhi(unsigned v) { return __uint_as_float(v & 0xffff0000u); }
DI f2 unp(unsigned v) { f2 r = {blo(v), bhi(v)}; return r; }
DI float fcos(float r) { return __builtin_amdgcn_cosf(r); }
DI float fsin(float r) { return __builtin_amdgcn_sinf(r); }
DI f4 mfma16(u4 a, u4 b, f4 c) { return __builtin_amdgcn_mfma_f32_16x16x32_bf16(__builtin_bit_cast(bf8, a), __builtin_bit_cast(bf8, b), c, 0, 0, 0); }
DI f16v mfma32(u4 a, u4 b, f16v c) { return __builtin_amdgcn_mfma_f32_32x32x16_bf16(__builtin_bit_cast(bf8, a), __builtin_bit_cast(bf8, b), c, 0, 0, 0); }
DI u2 trread(const char* p) { return __builtin_bit_cast(u2, __builtin_amdgcn_ds_read_tr16_b64_v4i16((LDS s4*)p)); }
DI u4 cat(u2 a, u2 b) { u4 r = {a[0], a[1], b[0], b[1]}; return r; }
DI rsrc mkrs(const void* p, unsigned bytes) { return __builtin_amdgcn_make_buffer_rsrc((void*)p, 0, bytes, 0x00020000); }
template <int AUX = 0> DI u4 ld128(rsrc r, int v, int s) { return __builtin_bit_cast(u4, __builtin_amdgcn_raw_buffer_load_b128(r, v, s, AUX)); }
template <int AUX = 0> DI unsigned ld32(rsrc r, int v, int s) { return __builtin_amdgcn_raw_buffer_load_b32(r, v, s, AUX); }
template <int AUX> DI void st128(u4 x, rsrc r, int v) { __builtin_amdgcn_raw_buffer_store_b128(x, r, v, 0, AUX); }
template <int AUX> DI void st32(unsigned x, rsrc r, int v, int s) { __builtin_amdgcn_raw_buffer_store_b32(x, r, v, s, AUX); }
DI unsigned and4(unsigned m, u4 a) { return m & a[0] & a[1] & a[2] & a[3]; }
DI void drain() { asm volatile("s_waitcnt vmcnt(0)" ::: "memory"); }
template <int H> DI f2 half(const f4& a) { return __builtin_shufflevector(a, a, 2 * H, 2 * H + 1); }
template <int H> DI f2 half(const f16v& a) { return __builtin_shufflevector(a, a, 2 * H, 2 * H + 1); }

DI void arrive(int off) {
  SCHED();
  asm volatile("s_mov_b64 exec, 1\n\tds_add_u32 %0, %1\n\ts_mov_b64 exec, -1" :: "v"(off), "v"(1u) : "memory");
  SCHED();
}
template <int SLEEP> DI void await(int off, int n) {
  unsigned v = 0, s;
  asm volatile("1:\n\tds_read_b32 %0, %2\n\ts_waitcnt lgkmcnt(0)\n\tv_readfirstlane_b32 %1, %0\n\ts_cmp_ge_u32 %1, %3\n\t"
               "s_cbranch_scc1 2f\n\ts_sleep %4\n\ts_branch 1b\n2:" : "+&v"(v), "=&s"(s) : "v"(off), "i"(n), "i"(SLEEP) : "memory", "scc");
}

constexpr int KCS = 4160;
constexpr int G_P = 4 * KCS, G_U = G_P + 16384;
constexpr int GRP = G_U + 4608;
constexpr int ZP = 768;
constexpr int L_SYNC = 0, L_TAB = 128, L_U = L_TAB + F_ALL * 1024, L_IMG = L_U + 12 * 1280, L_GRP = L_IMG + 10240, L_END = L_GRP + 3 * GRP;
enum { C_GOA, C_GOB, C_A1, C_A1D, C_B1, C_B1S, C_B1D, C_K, C_G = C_K + 3, C_A2D = C_G + 3, C_A3, C_B2D, C_PUBA, C_XI, C_Q, C_WL, C_TI, C_TAG = 31 };
#define SY(c) (L_SYNC + 4 * (c))

#define OPAQUE8(a) asm("" : "+v"((a)[0]), "+v"((a)[1]), "+v"((a)[2]), "+v"((a)[3]), "+v"((a)[4]), "+v"((a)[5]), "+v"((a)[6]), "+v"((a)[7]))
template <typename F> DI void dft64(const u4 (&ar)[4], const u4 (&ai)[4], const u4 (&b)[4], F f) {
  f4 er[4], ei[4];
  const f4 z = {0.f, 0.f, 0.f, 0.f};
#pragma unroll
  for (int c = 0; c < 4; ++c) { const int cc = (c >> 1) | ((c & 1) << 1); er[cc] = mfma16(ar[cc], b[cc], z); ei[cc] = mfma16(ai[cc], b[cc], z); }
#pragma unroll
  for (int rr = 0; rr < 4; ++rr) {
    const int r = (rr >> 1) | ((rr & 1) << 1);
    float t[8] = {er[0][r] + er[2][r], ei[0][r] + ei[2][r], er[0][r] - er[2][r], ei[0][r] - ei[2][r],
                  er[1][r] + er[3][r], ei[1][r] + ei[3][r], er[1][r] - er[3][r], ei[1][r] - ei[3][r]};
    OPAQUE8(t);
    float x[8] = {t[0] + t[4], t[1] + t[5], t[2] + t[7], t[3] - t[6], t[0] - t[4], t[1] - t[5], t[2] - t[7], t[3] + t[6]};
    OPAQUE8(x);
    f(r, x);
  }
}

template <typename F, int... I> DI void static_for_(F f, std::integer_sequence<int, I...>) { (f(std::integral_constant<int, I>{}), ...); }
template <int N, typename F> DI void static_for(F f) { static_for_(f, std::make_integer_sequence<int, N>{}); }
template <bool INV> DI void twiddle(const f16v& vr, const f16v& vi, const f4 (&tc)[3], const f4 (&tn)[3], u4& br, u4& bi, u4& bm) {
  unsigned pr[6], pi[6];
  f2 re[6], im[6];
  static_for<6>([&](auto jc) {
    constexpr int J = decltype(jc)::value;
    re[J] = half<J>(vi) * half<J & 1>(tn[J >> 1]); im[J] = half<J>(vr) * half<J & 1>(tn[J >> 1]);
  });
  SCHED();
  static_for<6>([&](auto jc) {
    constexpr int J = decltype(jc)::value;
    re[J] = __builtin_elementwise_fma(half<J>(vr), half<J & 1>(tc[J >> 1]), INV ? -re[J] : re[J]);
    im[J] = __builtin_elementwise_fma(half<J>(vi), half<J & 1>(tc[J >> 1]), INV ? im[J] : -im[J]);
  });
  SCHED();
#pragma unroll
  for (int j = 0; j < 6; ++j) { pr[j] = pk(re[j][0], re[j][1]); pi[j] = pk(im[j][0], im[j][1]); }
  br = (u4){pr[0], pr[1], pr[2], pr[3]}; bi = (u4){pi[0], pi[1], pi[2], pi[3]}; bm = (u4){pr[4], pr[5], pi[4], pi[5]};
}

struct ConvList { int n, a[36], b[36]; };
constexpr ConvList conv_list(int mask) {
  ConvList c{};
  for (int s = 0; s < 8; ++s) {
    for (int e = 0; e <= s; ++e) if (s + e < 8 && ((mask >> (s + e)) & 1)) { c.a[c.n] = s; c.b[c.n] = e; ++c.n; }
    for (int e = 0; e < s; ++e) if (s + e < 8 && ((mask >> (s + e)) & 1)) { c.a[c.n] = e; c.b[c.n] = s; ++c.n; }
  }
  return c;
}
constexpr int conv_rd(int n) {
  constexpr int RB[5] = {0, 4, 10, 14, 17};
  for (int f = 0; f < 5; ++f) if (n == RB[f]) return 3 * (3 + f);
  constexpr int RF[8] = {21, 24, 27, 32, 36, 39, 42, 45};
  for (int f = 0; f < 8; ++f) if (n >= RF[f] && n < RF[f] + 3) return 3 * (8 + f) + n - RF[f];
  return -1;
}
constexpr int conv_wr(int n) {
  constexpr int WF[7] = {49, 51, 53, 57, 63, 68, 71};
  for (int j = 0; j < 7; ++j) if (n == WF[j]) return j;
  return -1;
}
template <int MASK> DI void conv_part(char* base, int ao, int bo, int wo, int cg, const u4 (&a0)[8]) {
  constexpr int NB = (MASK & 0x80) ? 8 : 6;
  constexpr ConvList L = conv_list(MASK);
  constexpr bool FULL = MASK == 0xff;
  constexpr int PRO = FULL ? 3 : 4;
  f4 acc[8];
#pragma unroll
  for (int j = 0; j < 8; ++j) acc[j] = (f4){0.f, 0.f, 0.f, 0.f};
  u4 b[2 * NB]; u2 al[NB], ah[NB];
  auto rd = [&](int f, int part) {
    const int rr = f / NB, e = f % NB;
    if (part == 0) b[f] = *(const u4*)(base + bo + 512 * (7 - e) + 64 * rr);
    if (part == 1) al[e] = trread(base + ao + 512 * e + 64);
    if (part == 2) ah[e] = trread(base + ao + 512 * e + 64 + 8);
  };
  auto wr = [&](int j) { if ((MASK >> j) & 1) *(f4*)(base + wo + (j < 4 ? G_P + 4096 * j : 4096 * (j - 4))) = acc[j]; };
#pragma unroll
  for (int f = 0; f < PRO; ++f) { rd(f, 0); SCHED(); }
  constexpr int R1 = 9;
  constexpr int LASTRD = FULL ? 47 : R1 + (3 * NB - 1) / 2;
  unsigned seen = 0;
  static_for<2 * L.n>([&](auto nc) {
    constexpr int n = decltype(nc)::value, o = n < L.n ? 0 : NB, fa = L.a[n % L.n], fb = L.b[n % L.n];
    acc[fa + fb] = mfma16(n < L.n ? a0[fa] : cat(al[fa], ah[fa]), b[o + fb], acc[fa + fb]);
    SCHED();
    if constexpr (FULL) {
      constexpr int f = conv_rd(n), j = conv_wr(n);
      if constexpr (f >= 0) rd(f / 3, f % 3);
      if constexpr (n == 58) seen = *(volatile LDS unsigned*)(size_t)cg;
      if constexpr (j == 4) { if (__builtin_amdgcn_readfirstlane(seen) < 8) await<0>(cg, 8); }
      if constexpr (j >= 0) wr(j);
    } else {
      if constexpr (n < NB - PRO) rd(PRO + n, 0);
      constexpr int k = 2 * (n - R1);
      if constexpr (k >= 0 && k < 3 * NB) { rd(NB + k / 3, k % 3); rd(NB + (k + 1) / 3, (k + 1) % 3); }
    }
    if constexpr (n == LASTRD + 1) arrive(cg);
    SCHED();
  });
  if (FULL) wr(7);
  else {
    await<0>(cg, 8);
#pragma unroll
    for (int j = 0; j < 8; ++j) wr(j);
  }
}

template <bool STREAM> DI void glds16(const void* src, int dst) {
  unsigned keep;
  if (STREAM) asm volatile("s_mov_b32 %0, m0\n\ts_mov_b32 m0, %2\n\ts_nop 0\n\tglobal_load_lds_dwordx4 %1, off nt\n\ts_mov_b32 m0, %0"
                           : "=&s"(keep) : "v"(src), "s"(dst) : "memory");
  else asm volatile("s_mov_b32 %0, m0\n\ts_mov_b32 m0, %2\n\ts_nop 0\n\tglobal_load_lds_dwordx4 %1, off\n\ts_mov_b32 m0, %0"
                    : "=&s"(keep) : "v"(src), "s"(dst) : "memory");
}
DI unsigned now() { return (unsigned)__builtin_amdgcn_s_memrealtime(); }
DI void pace(unsigned t0, int x, int p) {
  const unsigned t1 = now();
  while ((int)(now() - t0) < x || (int)(now() - t1) < p) __builtin_amdgcn_s_sleep(1);
}
template <bool WIDE> DI void gather(rsrc rf, int off, unsigned tag) {
  SPIN_LOOP {
    CFENCE();
    bool ok;
    if (WIDE) { const u4 f = ld128<SC1>(rf, off, 0); ok = f[0] == tag && f[1] == tag && f[2] == tag && f[3] == tag; }
    else ok = ld32<SC1>(rf, off, 0) == tag;
    if (__all(ok)) break;
    __builtin_amdgcn_s_sleep(2);
  }
}

DI int fresh_lane() {
  unsigned z = 0;
  PIN(z);
  return __builtin_amdgcn_mbcnt_hi(~0u, __builtin_amdgcn_mbcnt_lo(~0u, z));
}
#define LANES() const int l = fresh_lane(), tid = 64 * w + l, i = l & 15, g = l >> 4, n32 = l & 31, h = l >> 5; char* tab = smem + L_TAB + 16 * l; \
  (void)tid; (void)i; (void)g; (void)n32; (void)h; (void)tab

extern "C" __global__ __launch_bounds__(1024, 4) void gfh3(const uint16_t* __restrict__ x, const uint16_t* __restrict__ wr, const uint16_t* __restrict__ wi,
                                                        const uint16_t* __restrict__ qin, const uint16_t* __restrict__ kin, const uint16_t* __restrict__ vin,
                                                        const uint16_t* __restrict__ shk, const uint16_t* __restrict__ ssk, const uint16_t* __restrict__ ssd,
                                                        uint16_t* __restrict__ out, char* ws) {
  __shared__ __attribute__((aligned(16))) char smem[L_END];
  const int l = threadIdx.x & 63;
  const int wv0 = (__builtin_amdgcn_readfirstlane(threadIdx.x >> 6) + 8) & 15;
  const int w = wv0 >= 12 ? wv0 ^ 1 : wv0;
  const int tid = 64 * w + l;
  const int xcd = blockIdx.x & 7, idx = blockIdx.x >> 3;
  const int q = idx & 3, s2 = xcd * 8 + (idx >> 2);
  const int rb = 8 * xcd + (idx >> 2), cgp = idx & 3;
  const int i = l & 15, g = l >> 4, n32 = l & 31, h = l >> 5;
  char* tab = smem + L_TAB + 16 * l;
#define FRAG(f) (*(u4*)(tab + 1024 * (f)))
#define FRAGF(f) (*(f4*)(tab + 1024 * (f)))
  const unsigned tstart = now();
  unsigned sink = 0;
  if (w == 8) {
    unsigned long long pc; int rel, len;
    asm volatile("s_getpc_b64 %0\n\ts_mov_b32 %1, gfh3@rel32@lo+4\n\ts_mov_b32 %2, gfh3_code_end-gfh3" : "=s"(pc), "=s"(rel), "=s"(len));
    const char* p = (const char*)(pc + (long long)rel) + min(128 * (8 * l + (idx & 7)), len);
    asm volatile("global_load_dword %0, %1, off" : "+v"(sink) : "v"(p) : "memory");
  }
  asm volatile("" :: "s"(x), "s"(wr), "s"(wi), "s"(qin), "s"(kin), "s"(vin), "s"(shk), "s"(ssk), "s"(ssd), "s"(out), "s"(ws));
  const rsrc rf = mkrs(ws + WS_FLAG, FL_EPOCH + 4);
  const int gi = w < 8 ? 2 : (w - 8) >> 2;
  bool conv = false;

  if (__builtin_expect((unsigned)(w - 8) < 5u, 1)) {
    const int t = tid - 512, c2 = t % 80, tc = t / 80;
    unsigned kr[11], vv[8], epoch;
    u4 wk;
    {
      const rsrc rk = mkrs(kin, 2048 * 1280), rv = mkrs(vin, 2048 * 1280), rs = mkrs(shk, 640 * 8);
      const int vo = (32 * rb + 8 * tc) * 1280 + 320 * cgp + 4 * c2;
      const int vh = (rb | tc) ? vo - 3 * 1280 : 2048 * 1280;
      epoch = ld32<SC1>(rf, w == 8 ? FL_EPOCH : FL_EPOCH + 4, 0);
      SCHED();
#pragma unroll
      for (int j = 0; j < 8; ++j) { kr[3 + j] = ld32<NT>(rk, vo, j * 1280); vv[j] = ld32<NT>(rv, vo, j * 1280); }
      wk = ld128(rs, 1280 * cgp + 16 * c2, 0);
#pragma unroll
      for (int j = 0; j < 3; ++j) kr[j] = ld32<NT>(rk, vh + j * 1280, 0);
      SCHED();
    }
    const rsrc ro = mkrs(ws + WS_KV + 192 * cgp * CP + 4032 - 64 * rb, 191 * CP + 64);
    int so = (t >> 2) * CP + 16 * (t & 3), ri[3];
#pragma unroll
    for (int j = 0; j < 3; ++j) {
      const int r = min((t >> 2) + 80 * j, 191), ch = r - (r + 3) / 6;
      ri[j] = L_IMG + (ch >> 1) * 128 + 16 * ((4 * (ch & 1) + (t & 3) + (ch >> 1)) & 7);
      PIN(ri[j]);
    }
    int wi0 = L_IMG + c2 * 128 + 16 * ((3 - tc + c2) & 7);
    PIN(so); PIN(wi0);
    SCHED();
    asm volatile("" : "+v"(sink), "+v"(epoch));
    if (w == 8) {
      *(unsigned*)(smem + SY(C_TAG)) = epoch * 0x9E3779B1u + 0x7F4A7C15u;
    }
    LBAR();
    __builtin_amdgcn_s_setprio(3);
    {
      const f2 w0 = {blo(wk[0]), blo(wk[2])}, w1 = {bhi(wk[0]), bhi(wk[2])}, w2 = {blo(wk[1]), blo(wk[3])}, w3 = {bhi(wk[1]), bhi(wk[3])};
      f2 kf[11], o[8];
#pragma unroll
      for (int j = 0; j < 11; ++j) kf[j] = unp(kr[j]);
#pragma unroll
      for (int j = 0; j < 8; ++j) o[j] = (kf[j] * w3 + kf[j + 1] * w2 + kf[j + 2] * w1 + kf[j + 3] * w0) * unp(vv[j]);
#pragma unroll
      for (int e = 0; e < 2; ++e) {
        const u4 r = {pk(o[7][e], o[6][e]), pk(o[5][e], o[4][e]), pk(o[3][e], o[2][e]), pk(o[1][e], o[0][e])};
        *(u4*)(smem + (wi0 ^ (64 * e))) = r & ~1u;
      }
    }
    arrive(SY(C_B1));
    await<0>(SY(C_B1), 5);
#pragma unroll
    for (int j = 0; j < 3; ++j) st128<SC1>(*(const u4*)(smem + ri[j]), ro, so + j * 80 * CP);
    arrive(SY(C_B1S));
    conv = true;
  } else if (__builtin_expect(w < 5, 1)) {
    u4 xv[8];
    const rsrc rx = mkrs(x + s2 * 640 + 128 * w, 0x7fffffff);
    const int vx = g * (512 * 1280) + 16 * i;
#pragma unroll
    for (int j = 0; j < 2; ++j) xv[j] = ld128(rx, vx, (j + 2 * q) * 64 * 1280);
    SCHED();
    u4 m1;
    {
      const int wv = i >> 1;
      const float kf = (wv < 4 ? (float)(4 * q + wv + 1) : 0.f) * (1.f / 2048.f);
      const float ph = 0.25f * (float)(i & 1);
#pragma unroll
      for (int jj = 0; jj < 4; ++jj) {
        const float sb = (float)(512 * g + s2 + 128 * ((jj + q) & 3));
        m1[jj] = pk(fcos(fmaf(kf, sb, ph)), fcos(fmaf(kf, sb + 64.f, ph)));
      }
    }
    const int ks1 = w < 4 ? 4 * q + w + 1 : 0;
    const rsrc rt = mkrs(ws + WS_T + s2 * TP * 4, 32 * SLOT * 4);
    const int cs = h ? (16 - ks1) * SLOT + (ks1 ? 0 : TP) : (ks1 + 16) * SLOT;
    int so = 4 * (cs + (h ? (32 - n32) & 31 : n32));
    int sx = 4 * (cs + 320 + n32 + (n32 < 16 ? 0 : TP));
    int upo = L_U + 2 * w * 1280 + 40 * (8 * h + ((l >> 2) & 3)) + 8 * min(4 * ((l >> 4) & 1) + (l & 3), 4);
    int wro = L_U + 4 * g * 1280 + 256 * w + 16 * i;
    PIN(m1); PIN(so); PIN(sx); PIN(upo); PIN(wro);
    BAR();
#pragma unroll
    for (int j = 2; j < 8; ++j) xv[j] = ld128(rx, vx, ((j + 2 * q) & 7) * 64 * 1280);
    arrive(SY(C_XI));
    u4 C0 = FRAG(F_C0), S0 = FRAG(F_S0);
    f4 tc[3], tn[3];
#pragma unroll
    for (int j = 0; j < 3; ++j) { tc[j] = FRAGF(F_TC + j); PIN(tc[j]); }
    const unsigned sg = (n32 & 1) ? SGN2 : 0u;
    u4 N0 = S0 ^ SGN2, C1 = C0 ^ sg, S1 = S0 ^ sg, N1 = N0 ^ sg;
    PIN(C0); PIN(S0); PIN(N0); PIN(C1); PIN(S1); PIN(N1);
    SCHED();
    __builtin_amdgcn_s_setprio(3);
    {
      f4 acc[8];
      u4 b[8];
#pragma unroll
      for (int jj = 0; jj < 4; ++jj) {
#pragma unroll
        for (int t = 0; t < 8; ++t) {
          b[t][jj] = __builtin_amdgcn_perm(xv[2 * jj + 1][t >> 1], xv[2 * jj][t >> 1], (t & 1) ? 0x07060302u : 0x05040100u);
          const f4 z = {0.f, 0.f, 0.f, 0.f};
          if (jj == 3) acc[t] = mfma16(m1, b[t], z);
        }
        SCHED();
      }
      if (g < 3) {
#pragma unroll
        for (int r = 0; r < 4; ++r) {
          u4 o = {pk(acc[0][r], acc[1][r]), pk(acc[2][r], acc[3][r]), pk(acc[4][r], acc[5][r]), pk(acc[6][r], acc[7][r])};
          *(u4*)(smem + wro + r * 1280) = o;
        }
      }
    }
    arrive(SY(C_A1));
    if (w < 4 || q == 0) {
      const u4 PC = FRAG(F_PC), PS = FRAG(F_PS), MR = FRAG(F_MR);
#pragma unroll
      for (int j = 0; j < 3; ++j) tn[j] = FRAGF(F_TS + j);
      await<0>(SY(C_A1), 5);
      const char* up = smem + upo;
      u4 are[2], aim[2];
#pragma unroll
      for (int kk = 0; kk < 2; ++kk) {
        are[kk] = cat(trread(up + 640 * kk), trread(up + 640 * kk + 160));
        aim[kk] = cat(trread(up + 1280 + 640 * kk), trread(up + 1280 + 640 * kk + 160));
      }
      f16v vr = {}, vi = {};
      vr = mfma32(are[0], C0, vr); vi = mfma32(are[0], N0, vi);
      vr = mfma32(aim[0], S0, vr); vi = mfma32(aim[0], C0, vi);
      vr = mfma32(are[1], C1, vr); vi = mfma32(are[1], N1, vi);
      vr = mfma32(aim[1], S1, vr); vi = mfma32(aim[1], C1, vi);
      const u4 PN = PS ^ SGN2, MI = {MR[2] ^ SGN2, MR[3] ^ SGN2, MR[0], MR[1]};
      u4 br, bi, bm;
      twiddle<false>(vr, vi, tc, tn, br, bi, bm);
      f16v xr = {}, xi = {};
      xr = mfma32(PC, br, xr); xi = mfma32(PN, br, xi);
      xr = mfma32(PS, bi, xr); xi = mfma32(PC, bi, xi);
      xr = mfma32(MR, bm, xr); xi = mfma32(MI, bm, xi);
      const unsigned cj = h ? 0x80000000u : 0u;
      unsigned o[10];
#pragma unroll
      for (int r = 0; r < 10; ++r) o[r] = (pk(xr[r], xi[r]) ^ cj) & ~1u;
      const auto sw = __builtin_amdgcn_permlane32_swap(o[9], o[0], false, false);
#pragma unroll
      for (int r = 1; r < 11; ++r) st32<SC1>(l == 32 ? (r < 10 ? o[r - 1] : sw[0] ^ 0x80000000u) : o[r % 10], rt, so + 128 * (r % 10), 0);
      st32<SC1>(h ? o[9] : sw[1] ^ 0x80000000u, rt, sx, 0);
    }
    if (w == 4) {
      LANES();
      const char* qp[10];
#pragma unroll
      for (int j = 0; j < 10; ++j) {
        const int c = l + 64 * j;
        qp[j] = (const char*)(qin + (32 * rb + c / 20) * 640 + 160 * cgp + 8 * (c % 20));
        PIN(qp[j]);
      }
      drain();
      arrive(SY(C_A1D));
      const unsigned tag = *(const unsigned*)(smem + SY(C_TAG));
      await<1>(SY(C_A1D), 5);
      if (l < 8) st32<SC1>(tag, rf, FL_A1 + 1024 * l + 4 * blockIdx.x, 0);
      arrive(SY(C_PUBA));
      await<1>(SY(C_B1D), 5);
      pace(tstart, XA, 35);
      gather<true>(rf, FL_A1 + 1024 * xcd + 16 * l, tag);
      arrive(SY(C_GOA));
      if (idx < 28) await<0>(SY(C_TI), 3);
#pragma unroll
      for (int j = 0; j < 10; ++j) glds16<true>(qp[j], L_IMG + 1024 * j);
      drain();
      arrive(SY(C_Q));
      if (blockIdx.x == 255 && l == 0) st32<SC1>(tag, rf, FL_EPOCH, 0);
    }
    conv = w < 4;
  } else if (w < 8) {
    const bool act = idx < 28;
    const int slot = 4 * xcd + idx / 7, tile = 3 * (idx % 7) + (w - 5);
    if (w < 7) {
      const int ro = (n32 & 3) + 4 * (n32 >> 3);
      const float a20 = w == 5 ? (float)((n32 & 4) ? 19 - ro : ro) * 0.05f : (float)n32 * -0.05f;
      u4 c, s, m;
#pragma unroll
      for (int jj = 0; jj < 4; ++jj) {
        const float a = a20 * (float)(8 * (jj >> 1) + 4 * h + 2 * (jj & 1)), b = a + a20;
        c[jj] = pk(fcos(a), fcos(b)); s[jj] = pk(fsin(a), fsin(b));
      }
#pragma unroll
      for (int jj = 0; jj < 2; ++jj) {
        const float a = a20 * (float)(16 + 2 * jj), b = a + a20;
        m[jj] = h ? 0u : pk(fcos(a), fcos(b)); m[2 + jj] = h ? 0u : pk(fsin(a), fsin(b));
      }
      char* tp = tab + (w - 5) * 3072;
      *(u4*)(tp + 1024 * F_PC) = c; *(u4*)(tp + 1024 * F_PS) = s; *(u4*)(tp + 1024 * F_MR) = m;
    } else {
      const float a64 = (float)i * (1.f / 64.f);
#pragma unroll
      for (int c = 0; c < 4; ++c) {
        u4 v;
#pragma unroll
        for (int u = 0; u < 4; ++u) { const float a = a64 * (float)(16 * u + c + 4 * g); v[u] = pk(fcos(a), fsin(a)); }
        FRAG(F_AR + c) = v;
      }
    }
    LBAR();
    u4 ar[4], ai[4];
#pragma unroll
    for (int c = 0; c < 4; ++c) {
      ar[c] = FRAG(F_AR + c);
#pragma unroll
      for (int u = 0; u < 4; ++u) ai[c][u] = __builtin_amdgcn_alignbit(ar[c][u], ar[c][u], 16) ^ 0x8000u;
      PIN(ar[c]); PIN(ai[c]);
    }
    const rsrc rt = mkrs(ws + WS_T, 64 * TP * 4);
    int vo = 4 * (4 * g * TP + slot * SLOT + 16 * tile + i);
    const int w0 = (slot - 16) * 321 + 48 * (idx % 7);
    const char* gp[5];
#pragma unroll
    for (int j = 0; j < 5; ++j) {
      const int n = min(3 * j + (w - 5), 13), c = l + 64 * (n % 7);
      int o = 2 * ((w0 & ~7) + (c / 7) * NCOL) + 16 * (c % 7);
      o += (o < 0) ? 128 * NCOL : 0;
      gp[j] = (const char*)(n < 7 ? wr : wi) + min(o, 128 * NCOL - 16);
      PIN(gp[j]);
    }
    PIN(vo);
    if (w < 7) {
      int o = 2 * (1284 * xcd - 16 * 321 - 16 + (2 * idx + h) * NCOL) + 128 * n32;
      o += (o < 0) ? 128 * NCOL : 0;
      await<1>(SY(C_B1S), 5);
      const unsigned t0 = ld32(mkrs(w == 5 ? wr : wi, 128 * NCOL), n32 < 22 ? o : 128 * NCOL, 0);
      asm volatile("" :: "v"(t0));
    }
    await<1>(SY(C_PUBA), 1);
    __builtin_amdgcn_s_setprio(3);
    if (act) {
#pragma unroll
      for (int j = 0; j < 5; ++j)
        if (j < 4 || w < 7) glds16<false>(gp[j], L_U + 1024 * (3 * j + (w - 5)));
    }
    drain();
    arrive(SY(C_WL));
    await<0>(SY(C_WL), 3);
    f2 wa[4][2], wb[4][2];
    const int kd = 16 * tile + i;
    const float wsc2 = (kd == 0 || kd == 320) ? 0.5f : 1.f;
#pragma unroll
    for (int u = 0; u < 4; ++u) {
#pragma unroll
      for (int r = 0; r < 4; ++r) {
        const char* p = smem + L_U + 448 * g + 2 * (i + 16 * (w - 5) + (w0 & 7)) + 1792 * u + 112 * r;
        wa[u][r >> 1][r & 1] = blo(*(const uint16_t*)p); wb[u][r >> 1][r & 1] = blo(*(const uint16_t*)(p + 7168));
      }
      PIN(wa[u][0]); PIN(wb[u][0]); PIN(wa[u][1]); PIN(wb[u][1]);
    }
    if (tile == 0 || tile == 20) {
#pragma unroll
      for (int e = 0; e < 16; ++e) {
        float ta = wa[e >> 2][(e >> 1) & 1][e & 1] * wsc2, tb = wb[e >> 2][(e >> 1) & 1][e & 1] * wsc2;
        asm("" : "+v"(ta), "+v"(tb));
        wa[e >> 2][(e >> 1) & 1][e & 1] = ta; wb[e >> 2][(e >> 1) & 1][e & 1] = tb;
      }
    }
    await<0>(SY(C_GOA), 1);
    if (act) {
      u4 b[4];
#pragma unroll
      for (int c = 0; c < 4; ++c) {
        const int cc = (c >> 1) | ((c & 1) << 1);
#pragma unroll
        for (int u = 0; u < 4; ++u) b[cc][u] = ld32<SC1>(rt, vo, (16 * u + cc) * TP * 4);
      }
      arrive(SY(C_TI));
      u4 b2[4];
      dft64(ar, ai, b, [&](int r, float (&x)[8]) {
        float y[8];
#pragma unroll
        for (int u = 0; u < 4; ++u) {
          const float a = wa[u][r >> 1][r & 1], c = wb[u][r >> 1][r & 1];
          y[2 * u] = fmaf(x[2 * u], a, -(x[2 * u + 1] * c)); y[2 * u + 1] = fmaf(-x[2 * u], c, -(x[2 * u + 1] * a));
        }
        OPAQUE8(y);
#pragma unroll
        for (int u = 0; u < 4; ++u) b2[r][u] = pk(y[2 * u], y[2 * u + 1]);
      });
      dft64(ar, ai, b2, [&](int r, float (&x)[8]) {
#pragma unroll
        for (int u = 0; u < 4; ++u) st32<SC1>(pk(x[2 * u], x[2 * u + 1]) | 1u, rt, vo, (16 * u + r) * TP * 4);
      });
    } else {
      __builtin_amdgcn_s_sleep(40);
    }
    __builtin_amdgcn_s_setprio(0);
    arrive(SY(C_A2D));
  } else {
    if (w == 15) {
      if (l < 31) *(unsigned*)(smem + L_SYNC + 4 * l) = 0u;
      unsigned zero = 0;
      PIN(zero);
      if (l == 0 && blockIdx.x < 72)
        __hip_atomic_fetch_or((GLB unsigned*)(ws + WS_FLAG + 128 * blockIdx.x), zero, __ATOMIC_RELAXED, __HIP_MEMORY_SCOPE_AGENT);
    }
    if (w == 13) {
      const float kf = (float)n32, a32 = kf * (1.f / 32.f), a640 = kf * (1.f / 640.f);
      u4 c, s;
#pragma unroll
      for (int jj = 0; jj < 4; ++jj) {
        const float a = a32 * (float)(8 * h + 2 * jj), b = a + a32;
        c[jj] = pk(fcos(a), fcos(b)); s[jj] = pk(fsin(a), fsin(b));
      }
      FRAG(F_C0) = c; FRAG(F_S0) = s;
#pragma unroll
      for (int rg = 0; rg < 3; ++rg) {
        f4 tc, tn;
#pragma unroll
        for (int jj = 0; jj < 4; ++jj) { const float a = a640 * (float)(jj + 8 * rg + 4 * h); tc[jj] = fcos(a); tn[jj] = fsin(a); }
        FRAGF(F_TC + rg) = tc; FRAGF(F_TS + rg) = tn;
      }
    }
    if (w == 14) {
      u4 m3[2];
      const float scale = 2.f / (2048.f * 640.f);
      const float sf = (float)(64 * (8 * q + (i >> 1)) + s2) * (1.f / 2048.f);
      const float ph = -0.25f * (float)(i & 1);
#pragma unroll
      for (int j = 0; j < 8; ++j) {
        const int jr = (j + 2 * q) & 7;
        const float a = fmaf(sf, (float)(16 * (jr >> 2) + (jr & 3) - 16 + 4 * g), ph);
        m3[j >> 2][j & 3] = pk(fcos(a) * scale, fsin(a) * scale);
      }
      FRAG(F_M3) = m3[0]; FRAG(F_M3 + 1) = m3[1];
    }
    LBAR();
    await<0>(SY(C_XI), 5);
    const int gk = w - 13, ck = 80 * xcd + 5 * (idx >> 1) + (gk == 2 ? 2 : gk + 3 * (idx & 1));
    const rsrc rk = mkrs(ssk + ck * 2048, 4096);
    const unsigned dk = __builtin_amdgcn_raw_buffer_load_b16(mkrs(ssd, 1280), 2 * ck, 0, 0);
    u4 kc[4]; u2 kp[4];
#pragma unroll
    for (int j = 0; j < 4; ++j) kc[j] = ld128<NT>(rk, 16 * l + 1024 * j, 0);
    SCHED();
#pragma unroll
    for (int j = 0; j < 4; ++j)
#pragma unroll
      for (int e = 0; e < 2; ++e) {
        const unsigned up = __builtin_amdgcn_update_dpp(0u, kc[j][2 + e], 0x138, 0xf, 0xf, false);
        kp[j][e] = l ? up : (j ? __builtin_amdgcn_readlane(kc[j - 1][2 + e], 63) : 0u);
      }
    if (l == 0) kc[0][0] = pk(blo(kc[0][0]) + blo(dk), bhi(kc[0][0]));
    char* base = smem + L_GRP + gk * GRP;
    const u4 z4 = {0u, 0u, 0u, 0u};
    if (l < 40) *(u4*)(base + (l < 8 ? (l >> 1) * KCS + 16 * (l & 1) : G_U + 4096 + 16 * (l - 8))) = z4;
#pragma unroll
    for (int j = 0; j < 4; ++j) {
      const unsigned o0 = __builtin_amdgcn_alignbit(kp[j][1], kp[j][0], 16), o1 = __builtin_amdgcn_alignbit(kc[j][0], kp[j][1], 16),
                     o2 = __builtin_amdgcn_alignbit(kc[j][1], kc[j][0], 16), o3 = __builtin_amdgcn_alignbit(kc[j][2], kc[j][1], 16),
                     o4 = __builtin_amdgcn_alignbit(kc[j][3], kc[j][2], 16);
      char* kp0 = base + 32 + 16 * l + 1024 * j;
      const u4 c1 = {o1, o2, o3, o4}, c2 = {kp[j][1], kc[j][0], kc[j][1], kc[j][2]}, c3 = {o0, o1, o2, o3};
      *(u4*)(kp0) = kc[j]; *(u4*)(kp0 + KCS) = c1; *(u4*)(kp0 + 2 * KCS) = c2; *(u4*)(kp0 + 3 * KCS) = c3;
    }
    arrive(SY(C_K + gk));
    conv = true;
  }

  if (conv) {
    LANES();
    const int wl = w & 3, t = tid & 255;
    char* base = smem + L_GRP + gi * GRP;
    char* ul = base + G_U;
    const rsrc ru = mkrs(ws + WS_KV + (96 * xcd + 3 * idx + (gi == 2 ? 2 - 2 * (idx & 1) : gi + (idx & 1))) * CP, 4096);
    int ao = (3 - (i >> 2)) * KCS + 16 * g + 8 * (i & 3) + 8 + 128 * wl;
    int bo = G_U + 4064 - 3584 - 32 * i + 16 * g + 128 * wl;
    int wo = wl * 1024 + 16 * (4 * i + ((g + (i >> 1)) & 3));
    const int c = (t >> 1) & 15, r0 = (wl < 2 ? G_P : -16384) + 4096 * (t >> 5) + 64 * c;
    int p0 = r0 + 16 * ((2 * (t & 1) + (c >> 1)) & 3), p1 = r0 + 16 * ((2 * (t & 1) + 1 + (c >> 1)) & 3);
    const int mask = gi < 2 ? 0xff : ((idx & 1) ? 0x3b : 0xc4);
    int yo = ((mask >> (t >> 5)) & 1) ? 4080 - 16 * t : 4096;
    PIN(ao); PIN(bo); PIN(wo); PIN(p0); PIN(p1); PIN(yo);
    if (w < 13) {
      drain();
      arrive(w < 4 ? SY(C_A1D) : SY(C_B1D));
    }
    if (w == 12) {
      const unsigned tag = *(const unsigned*)(smem + SY(C_TAG));
      await<0>(SY(C_B1D), 5);
      if (l == 0) st32<SC1>(tag, rf, FL_B1 + 256 * cgp + 4 * rb, 0);
      pace(tstart, XB, 35);
      gather<false>(rf, FL_B1 + 256 * (xcd >> 1) + 4 * l, tag);
      arrive(SY(C_GOB));
    }
    u4 a0[8];
    await<1>(SY(C_K + gi), 1);
#pragma unroll
    for (int e = 0; e < 8; ++e) { a0[e] = cat(trread(base + ao + 512 * e), trread(base + ao + 512 * e + 8)); PIN(a0[e]); }
    __builtin_amdgcn_s_setprio(3);
    await<1>(SY(C_GOB), 1);
    const u4 uv = ld128<SC1>(ru, 16 * t, 0);
    *(u4*)(ul + 16 * t) = uv;
    const int cg = SY(C_G + gi);
    arrive(cg);
    await<0>(cg, 4);
    if (gi == 2) __builtin_amdgcn_s_setprio(2);
    if (gi == 0) __builtin_amdgcn_s_setprio(1);
    if (gi == 1) __builtin_amdgcn_s_setprio(0);
    if (gi < 2) conv_part<0xff>(base, ao, bo, wo, cg, a0);
    else if (idx & 1) conv_part<0x3b>(base, ao, bo, wo, cg, a0);
    else conv_part<0xc4>(base, ao, bo, wo, cg, a0);
    arrive(cg);
    await<0>(cg, 12);
    f4 pa[4], pb[4];
#pragma unroll
    for (int ww = 0; ww < 4; ++ww) { pa[ww] = *(const f4*)(base + p0 + ww * 1024); pb[ww] = *(const f4*)(base + p1 + ww * 1024); }
    float sm[8], sn[8];
#pragma unroll
    for (int e = 0; e < 4; ++e) { sm[e] = pa[0][e] + pa[1][e]; sm[4 + e] = pb[0][e] + pb[1][e]; sn[e] = pa[2][e] + pa[3][e]; sn[4 + e] = pb[2][e] + pb[3][e]; }
    OPAQUE8(sm); OPAQUE8(sn);
#pragma unroll
    for (int e = 0; e < 8; ++e) sm[e] += sn[e];
    OPAQUE8(sm);
    const u4 o = {pk(sm[7], sm[6]), pk(sm[5], sm[4]), pk(sm[3], sm[2]), pk(sm[1], sm[0])};
    st128<SC1>(o | 1u, ru, yo);
    __builtin_amdgcn_s_setprio(0);
    arrive(SY(C_B2D));
  }

  if (w < 8) {
    LANES();
    u4 C0 = FRAG(F_C0), S0 = FRAG(F_S0), QC = FRAG(F_QC), QS = FRAG(F_QS), QM = FRAG(F_QM);
    f4 tc[3], tn[3];
#pragma unroll
    for (int j = 0; j < 3; ++j) { tc[j] = FRAGF(F_TC + j); tn[j] = FRAGF(F_TS + j); PIN(tc[j]); PIN(tn[j]); }
    u4 N0 = S0 ^ SGN2;
    PIN(C0); PIN(S0); PIN(N0); PIN(QC); PIN(QS); PIN(QM);
    int zo = L_U + 2 * w * ZP + 40 * (8 * h + ((l >> 2) & 3)) + 8 * min(4 * ((l >> 4) & 1) + (l & 3), 4);
    int zw = L_U + 2 * w * ZP + 72 * min(n32, 20) + 8 * h, zs = L_U + 2 * w * ZP + 72 * (l >> 2) + 16 * (l & 3);
    const rsrc ro = mkrs(out + (64 * (8 * q + w) + s2) * 1280, 1280);
    PIN(zo); PIN(zw); PIN(zs);
    asm volatile("" :: "s"(ro));
    if (w < 6) {
      u4 m3[2] = {FRAG(F_M3), FRAG(F_M3 + 1)};
      const rsrc rt = mkrs(ws + WS_T + s2 * TP * 4, TP * 4);
      int vo = 4 * (4 * g * SLOT + (w < 5 ? 64 * w + 4 * i : 320));
      int wo = L_U + 4 * g * ZP + 128 * w + 8 * i;
      PIN(m3[0]); PIN(m3[1]); PIN(vo); PIN(wo);
      await<2>(SY(C_A2D), 3);
      __builtin_amdgcn_s_sleep(D_X2);
      pace(tstart, X2A, 0);
      __builtin_amdgcn_s_setprio(3);
      const f4 z = {0.f, 0.f, 0.f, 0.f};
      bool bad = true;
      if (w < 5) {
        u4 tv[8];
        f4 acc[4];
        SPIN_LOOP {
          CFENCE();
          if (bad) {
#pragma unroll
            for (int j = 0; j < 8; ++j) { const int jr = (j + 2 * q) & 7; tv[j] = ld128<SC1>(rt, vo, 4 * ((jr & 3) + 16 * (jr >> 2)) * SLOT); }
          }
          unsigned m = 1u;
#pragma unroll
          for (int kk = 0; kk < 2; ++kk) {
#pragma unroll
            for (int t = 0; t < 4; ++t) {
              const u4 b = {tv[4 * kk][t], tv[4 * kk + 1][t], tv[4 * kk + 2][t], tv[4 * kk + 3][t]};
              acc[t] = mfma16(m3[kk], b, kk ? acc[t] : z);
              m = and4(m, b);
            }
            SCHED();
          }
          bad = m == 0u;
          if (!__any(bad)) break;
          __builtin_amdgcn_s_sleep(D_RETRY);
        }
#pragma unroll
        for (int r = 0; r < 4; ++r) {
          const u2 o = {pk(acc[0][r], acc[1][r]), pk(acc[2][r], acc[3][r])};
          *(u2*)(smem + wo + r * ZP) = o;
        }
      } else {
        u4 nv[2];
        SPIN_LOOP {
          CFENCE();
#pragma unroll
          for (int j = 0; j < 8; ++j) { const int jr = (j + 2 * q) & 7; nv[j >> 2][j & 3] = ld32<SC1>(rt, vo, 4 * ((jr & 3) + 16 * (jr >> 2)) * SLOT); }
          if (__all(and4(and4(1u, nv[0]), nv[1]) != 0u)) break;
          __builtin_amdgcn_s_sleep(D_RETRY);
        }
        const f4 acc = mfma16(m3[1], nv[1], mfma16(m3[0], nv[0], z));
        *(float*)(smem + wo) = acc[0]; *(float*)(smem + wo + 2 * ZP) = acc[2];
      }
      arrive(SY(C_A3));
    }
    await<0>(SY(C_A3), 6);
    {
      const char* zp = smem + zo;
      const u4 are = cat(trread(zp), trread(zp + 160)), aim = cat(trread(zp + ZP), trread(zp + ZP + 160));
      const float nyq = *(const float*)(smem + L_U + 2 * w * ZP + 640);
      f16v vr = {}, vi = {};
      vr = mfma32(are, C0, vr); vi = mfma32(are, S0, vi);
      vr = mfma32(aim, N0, vr); vi = mfma32(aim, C0, vi);
      f16v y;
#pragma unroll
      for (int r = 0; r < 16; ++r) y[r] = (r & 1) ? -nyq : nyq;
      u4 br, bi, bm;
      twiddle<true>(vr, vi, tc, tn, br, bi, bm);
      y = mfma32(br, QC, y); y = mfma32(bi, QS, y); y = mfma32(bm, QM, y);
#pragma unroll
      for (int t = 0; t < 4; ++t) {
        const u2 o = {pk(y[4 * t], y[4 * t + 1]), pk(y[4 * t + 2], y[4 * t + 3])};
        *(u2*)(smem + zw + 16 * t) = o;
      }
      CFENCE();
      const char* zq = smem + zs;
      const u4 o0 = cat(*(const u2*)zq, *(const u2*)(zq + 8)), o1 = cat(*(const u2*)(zq + 72 * 16), *(const u2*)(zq + 72 * 16 + 8));
      st128<SC1>(o0, ro, 16 * l);
      st128<SC1>(o1, ro, 16 * l + 1024);
    }
  } else if (w < 13) {
    LANES();
    const int t = tid - 512;
    constexpr int L_Y = L_GRP;
    u4 yv[2];
    const rsrc ry = mkrs(ws + WS_KV + 192 * cgp * CP + 4032 - 64 * rb, 192 * CP);
    int yi = ((t >> 2) + ((t >> 2) + 2 + ((0x3b >> xcd) & 1)) / 5) * CP + 16 * (t & 3);
    PIN(yi);
    await<2>(SY(C_B2D), 12);
    __builtin_amdgcn_s_sleep(D_X2);
    pace(tstart, X2B, 0);
    __builtin_amdgcn_s_setprio(3);
#pragma unroll
    for (int j = 0; j < 2; ++j) yv[j] = ld128<SC1>(ry, yi, j * 96 * CP);
    SCHED();
    const int c8 = t >> 4;
    await<0>(SY(C_Q), 1);
    f2 qf[2][4];
#pragma unroll
    for (int j = 0; j < 2; ++j) {
      const u4 qv = *(const u4*)(smem + L_IMG + (31 - i - 16 * j) * 320 + 16 * c8);
#pragma unroll
      for (int e = 0; e < 4; ++e) { qf[j][e] = unp(qv[e]); PIN(qf[j][e]); }
    }
    int wo = L_Y + (t >> 2) * 80 + 16 * (t & 3);
    int po = L_Y + (8 * c8 + (i >> 2)) * 80 + 8 * (i & 3), oo = (15 - (l >> 2)) * 2560 + 64 * (w - 8) + 16 * (l & 3);
    int xo = L_Y + 16384 + 2560 * (w - 8) + 80 * i + 16 * g, xi = L_Y + 16384 + 2560 * (w - 8) + 80 * (l >> 2) + 16 * (l & 3);
    const rsrc ro = mkrs(out + 32 * rb * 1280 + 640 + 160 * cgp, 32 * 2560);
    asm volatile("" :: "s"(ro));
    PIN(wo); PIN(po); PIN(oo); PIN(xo); PIN(xi);
    SCHED();
    SPIN_LOOP {
      const bool bad = and4(and4(1u, yv[0]), yv[1]) == 0u;
      if (!__any(bad)) break;
      __builtin_amdgcn_s_sleep(D_RETRY);
      CFENCE();
      if (bad) {
#pragma unroll
        for (int j = 0; j < 2; ++j) yv[j] = ld128<SC1>(ry, yi, j * 96 * CP);
      }
    }
#pragma unroll
    for (int j = 0; j < 2; ++j) *(u4*)(smem + wo + 80 * 80 * j) = yv[j];
    arrive(SY(C_B1));
    await<0>(SY(C_B1), 10);
    u4 o[2];
#pragma unroll
    for (int j = 0; j < 2; ++j) {
      const char* p = smem + po + 32 * j;
      const u4 y = cat(trread(p), trread(p + 4 * 80));
      float v[8];
#pragma unroll
      for (int e = 0; e < 4; ++e) { v[2 * e] = blo(y[e]) * qf[j][e][0]; v[2 * e + 1] = bhi(y[e]) * qf[j][e][1]; }
      OPAQUE8(v);
#pragma unroll
      for (int e = 0; e < 4; ++e) o[j][e] = pk(v[2 * e], v[2 * e + 1]);
      *(u4*)(smem + xo + 1280 * j) = o[j];
    }
    CFENCE();
#pragma unroll
    for (int j = 0; j < 2; ++j) st128<SC1>(*(const u4*)(smem + xi + 1280 * j), ro, oo + (1 - j) * 16 * 2560);
  }
}

extern "C" __device__ __attribute__((used, noinline)) void gfh3_code_end() {}

extern "C" void kernel_launch(void* const* d_in, const int* in_sizes, int n_in, void* d_out, int out_size, void* d_ws,
                              size_t ws_size, hipStream_t stream) {
  gfh3<<<256, 1024, 0, stream>>>((const uint16_t*)d_in[0], (const uint16_t*)d_in[1], (const uint16_t*)d_in[2], (const uint16_t*)d_in[3],
                                 (const uint16_t*)d_in[4], (const uint16_t*)d_in[5], (const uint16_t*)d_in[6], (const uint16_t*)d_in[7],
                                 (const uint16_t*)d_in[8], (uint16_t*)d_out, (char*)d_ws);
}
```
